```python
import torch

def _softmax(x, axis=-1):
    return torch.softmax(x, dim=axis)

def _gelu(x):
    return torch.nn.functional.gelu(x, approximate='tanh')

def _silu(x):
    return torch.nn.functional.silu(x)

def _sigmoid(x):
    return torch.sigmoid(x)

def _layernorm(x, axis=-1, eps=1e-05):
    m = x.mean(dim=axis, keepdim=True)
    v = x.var(dim=axis, keepdim=True, unbiased=False)
    return (x - m) / torch.sqrt(v + eps)

def _scatter_add(tgt, idx, src, axis=0):
    return tgt.clone().index_add_(axis, idx, src)

def _segment_sum(src, seg, n):
    out = torch.zeros((n,) + src.shape[1:], dtype=src.dtype)
    return out.index_add_(0, seg, src)

def _pad(x, pw):
    flat = tuple((v for pair in reversed(pw) for v in pair))
    return torch.nn.functional.pad(x, flat)
kernel_name = 'sequential_preLN__memory_aug_recurrent_mem_tr__geglu_ffn__prompt'

def setup_inputs():

    _w = lambda a: (lambda t: t.to(torch.bfloat16) if t.is_floating_point() else t)(torch.as_tensor(a))

    def _A_setup():
        B, S, Dm = (1, 2048, 512)
        H, Dh = (32, 128)
        M, seg = (8, 32)
        seg = min(seg, S)
        S = S // seg * seg
        return {'x': _w((lambda *s: torch.randn(*s, dtype=torch.bfloat16) * 0.05)(B, S, Dm)), 'mem0': _w((lambda *s: torch.randn(*s, dtype=torch.bfloat16) * 0.2)(B, M, Dm)), 'Wq': _w((lambda *s: torch.randn(*s, dtype=torch.bfloat16) * 1.6)(Dm, H, Dh) * Dm ** (-0.5)), 'Wk': _w((lambda *s: torch.randn(*s, dtype=torch.bfloat16) * 1.6)(Dm, H, Dh) * Dm ** (-0.5)), 'Wv': _w((lambda *s: torch.randn(*s, dtype=torch.bfloat16) * 0.2)(Dm, H, Dh) * Dm ** (-0.5)), 'Wo': _w((lambda *s: torch.randn(*s, dtype=torch.bfloat16) * 0.2)(H, Dh, Dm) * (H * Dh) ** (-0.5)), 'seg_len': seg}
    _A_in = _A_setup()

    def _B_setup():
        B, S, D, F = (1, 2048, 512, 1792)
        return {'x': _w((lambda *s: torch.randn(*s, dtype=torch.bfloat16) * 0.05)(B, S, D)), 'w_gate': _w((lambda *s: torch.randn(*s, dtype=torch.bfloat16) * 0.05)(D, F)), 'w_up': _w((lambda *s: torch.randn(*s, dtype=torch.bfloat16) * 0.05)(D, F)), 'w_down': _w((lambda *s: torch.randn(*s, dtype=torch.bfloat16) * 0.05)(F, D))}
    _B_in = _B_setup()
    return {'A_x': _A_in['x'], 'A_mem0': _A_in['mem0'], 'A_Wq': _A_in['Wq'], 'A_Wk': _A_in['Wk'], 'A_Wv': _A_in['Wv'], 'A_Wo': _A_in['Wo'], 'A_seg_len': _A_in['seg_len'], 'B_x': _B_in['x'], 'B_w_gate': _B_in['w_gate'], 'B_w_up': _B_in['w_up'], 'B_w_down': _B_in['w_down']}

def reference(A_x, A_mem0, A_Wq, A_Wk, A_Wv, A_Wo, A_seg_len, B_x, B_w_gate, B_w_up, B_w_down):

    def _A_ref(x, mem0, Wq, Wk, Wv, Wo, seg_len):
        B, S, Dm = x.shape
        H, Dh = (Wq.shape[1], Wq.shape[2])
        M = mem0.shape[1]
        scale = Dh ** (-0.5)
        n_seg = S // seg_len
        L = M + seg_len
        idx = torch.arange(L)
        mask = (idx[:, None] < M) | (idx[:, None] >= idx[None, :])
        mem = mem0
        outs = []
        for s in range(n_seg):
            xs = x[:, s * seg_len:(s + 1) * seg_len, :].clone()
            h = torch.concatenate([mem, xs], axis=1)
            q = torch.einsum('btd,dhe->bhte', h, Wq)
            k = torch.einsum('btd,dhe->bhte', h, Wk)
            v = torch.einsum('btd,dhe->bhte', h, Wv)
            scores = torch.einsum('bhqe,bhke->bhqk', q, k) * scale
            scores = torch.where(mask[None, None], scores, -1000000000.0)
            p = _softmax(scores, axis=-1)
            o = torch.einsum('bhqk,bhke->bhqe', p, v)
            o = torch.einsum('bhte,hed->btd', o, Wo)
            mem = o[:, :M, :].clone()
            outs.append(o[:, M:, :])
        return torch.concatenate(outs, axis=1)

    def _B_ref(x, w_gate, w_up, w_down):
        return _gelu(x @ w_gate) * (x @ w_up) @ w_down
    x = A_x
    h = x + _A_ref(_layernorm(x, axis=-1), A_mem0, A_Wq, A_Wk, A_Wv, A_Wo, A_seg_len)
    h2 = h + _B_ref(_layernorm(h, axis=-1), B_w_gate, B_w_up, B_w_down)
    return h2
```

```cpp
#include <hip/hip_runtime.h>
#include <hip/hip_bf16.h>
#include <stdint.h>

typedef unsigned short u16;
typedef __bf16 bf16x8 __attribute__((ext_vector_type(8)));
typedef __bf16 bf16x4 __attribute__((ext_vector_type(4)));
typedef __bf16 bf16x16 __attribute__((ext_vector_type(16)));
typedef short s16x4 __attribute__((ext_vector_type(4)));
typedef float f32x4 __attribute__((ext_vector_type(4)));
typedef unsigned v4u __attribute__((ext_vector_type(4)));
typedef unsigned v2u __attribute__((ext_vector_type(2)));
#define LAS __attribute__((address_space(3)))

constexpr int S_LEN = 2048, DM = 512, NH = 32, DH = 128, HD = NH * DH, MM = 8, SEG = 32, NSEG = S_LEN / SEG, FF = 1792;

constexpr size_t WS_RS = 0;
constexpr size_t WS_AG = WS_RS + 2u * 32 * 32 * 512;
constexpr size_t WS_SYNC_END = WS_AG + 2u * 8192;
constexpr size_t WS_XN = 2u << 20;
constexpr size_t WS_Q = WS_XN + (size_t)S_LEN * DM * 2;
constexpr size_t WS_K = WS_Q + (size_t)S_LEN * HD * 2;
constexpr size_t WS_V = WS_K + (size_t)S_LEN * HD * 2;
constexpr size_t WS_O = WS_V + (size_t)S_LEN * HD * 2;
constexpr size_t WS_KM = WS_O + (size_t)S_LEN * HD * 2;
constexpr size_t WS_VM = WS_KM + (size_t)NSEG * NH * MM * DH * 2;
constexpr size_t WS_H = WS_VM + (size_t)NSEG * NH * MM * DH * 2;
constexpr size_t WS_HN = WS_H + (size_t)S_LEN * DM * 2;
constexpr size_t WS_ACT = WS_HN + (size_t)S_LEN * DM * 2;
constexpr size_t WS_END = WS_ACT + (size_t)S_LEN * FF * 2;

__device__ __forceinline__ float bf2f(u16 v) { return __uint_as_float((unsigned)v << 16); }
__device__ __forceinline__ u16 f2bf(float f) { return __builtin_bit_cast(u16, (__bf16)f); }
__device__ __forceinline__ float rbf(float f) { return (float)(__bf16)f; }
__device__ __forceinline__ unsigned pk2(float lo, float hi) { return (unsigned)f2bf(lo) | ((unsigned)f2bf(hi) << 16); }
__device__ __forceinline__ s16x4 tr_read(const LAS unsigned char* p) {
    return __builtin_amdgcn_ds_read_tr16_b64_v4i16((LAS s16x4*)p);
}
__device__ __forceinline__ bf16x8 cat8(s16x4 a, s16x4 b) {
    typedef short s16x8 __attribute__((ext_vector_type(8)));
    s16x8 r = __builtin_shufflevector(a, b, 0, 1, 2, 3, 4, 5, 6, 7);
    return __builtin_bit_cast(bf16x8, r);
}
__device__ __forceinline__ bf16x16 cat16(bf16x8 a, bf16x8 b) {
    return __builtin_shufflevector(a, b, 0, 1, 2, 3, 4, 5, 6, 7, 8, 9, 10, 11, 12, 13, 14, 15);
}
#define MFMA32(a, b, c) __builtin_amdgcn_mfma_f32_16x16x32_bf16((a), (b), (c), 0, 0, 0)
#define SMFMAC(a, b, c, idx) __builtin_amdgcn_smfmac_f32_16x16x64_bf16((a), (b), (c), (idx), 0, 0)

__global__ void __launch_bounds__(256) k_ln(const u16* __restrict__ x, u16* __restrict__ y) {
    const int row = blockIdx.x * 4 + (threadIdx.x >> 6), lane = threadIdx.x & 63;
    const v4u raw = *(const v4u*)(x + (size_t)row * DM + lane * 8);
    float v[8];
#pragma unroll
    for (int i = 0; i < 4; ++i) { v[2 * i] = __uint_as_float(raw[i] << 16); v[2 * i + 1] = __uint_as_float(raw[i] & 0xffff0000u); }
    float s = 0.f;
#pragma unroll
    for (int i = 0; i < 8; ++i) s += v[i];
#pragma unroll
    for (int o = 32; o >= 1; o >>= 1) s += __shfl_xor(s, o);
    const float mean = s * (1.0f / DM);
    float q = 0.f;
#pragma unroll
    for (int i = 0; i < 8; ++i) { const float d = v[i] - mean; q += d * d; }
#pragma unroll
    for (int o = 32; o >= 1; o >>= 1) q += __shfl_xor(q, o);
    const float mb = rbf(mean), vb = rbf(q * (1.0f / DM));
    const float den = rbf(sqrtf(rbf(vb + 1e-5f)));
    v4u o;
#pragma unroll
    for (int i = 0; i < 4; ++i) o[i] = pk2(rbf(v[2 * i] - mb) / den, rbf(v[2 * i + 1] - mb) / den);
    *(v4u*)(y + (size_t)row * DM + lane * 8) = o;
}

constexpr int G_BM = 128, G_BN = 128, G_BK = 64;
constexpr int G_AST = 144, G_BST = 288;
constexpr int G_ABYTES = G_BM * G_AST, G_BBYTES = G_BK * G_BST, G_STAGE = G_ABYTES + G_BBYTES;

template <int EPI>
__global__ void __launch_bounds__(256) k_gemm(const u16* __restrict__ A, int lda, const u16* __restrict__ B, const u16* __restrict__ B2, int ldb,
                                              u16* __restrict__ C, int ldc, const u16* __restrict__ res, int K) {
    extern __shared__ __attribute__((aligned(16))) unsigned char smem[];
    const int t = threadIdx.x, w = t >> 6, l = t & 63, g = l >> 4, c16 = l & 15;
    const int wr = w >> 1, wc = w & 1;
    const int m0 = blockIdx.y * G_BM;
    const int n0 = blockIdx.x * (EPI == 2 ? 64 : G_BN);
    v4u ra[4], rb[4];
    auto gload = [&](int k0) {
#pragma unroll
        for (int i = 0; i < 4; ++i) {
            const int u = i * 256 + t;
            ra[i] = *(const v4u*)(A + (size_t)(m0 + (u >> 3)) * lda + k0 + (u & 7) * 8);
            const int kr = u >> 4, ch = u & 15;
            if (EPI == 2) {
                const int wcc = ch >> 3, part = (ch >> 2) & 1, cc = (ch & 3) * 8;
                const u16* src = part ? B2 : B;
                rb[i] = *(const v4u*)(src + (size_t)(k0 + kr) * ldb + n0 + 32 * wcc + cc);
            } else {
                rb[i] = *(const v4u*)(B + (size_t)(k0 + kr) * ldb + n0 + ch * 8);
            }
        }
    };
    auto swrite = [&](int buf) {
        unsigned char* sa = smem + buf * G_STAGE; unsigned char* sb = sa + G_ABYTES;
#pragma unroll
        for (int i = 0; i < 4; ++i) {
            const int u = i * 256 + t;
            *(v4u*)(sa + (u >> 3) * G_AST + (u & 7) * 16) = ra[i];
            *(v4u*)(sb + (u >> 4) * G_BST + (u & 15) * 16) = rb[i];
        }
    };
    f32x4 acc[4][4];
#pragma unroll
    for (int i = 0; i < 4; ++i)
#pragma unroll
        for (int j = 0; j < 4; ++j) acc[i][j] = (f32x4){0.f, 0.f, 0.f, 0.f};
    const int nk = K / G_BK;
    gload(0); swrite(0);
    __syncthreads();
    for (int kt = 0; kt < nk; ++kt) {
        const int buf = kt & 1;
        if (kt + 1 < nk) gload((kt + 1) * G_BK);
        const LAS unsigned char* sa = (const LAS unsigned char*)smem + buf * G_STAGE;
        const LAS unsigned char* sb = sa + G_ABYTES;
#pragma unroll
        for (int ks = 0; ks < 2; ++ks) {
            bf16x8 af[4], bfr[4];
#pragma unroll
            for (int i = 0; i < 4; ++i) af[i] = *(const LAS bf16x8*)(sa + (wr * 64 + i * 16 + c16) * G_AST + (ks * 32 + g * 8) * 2);
#pragma unroll
            for (int j = 0; j < 4; ++j) {
                const LAS unsigned char* p = sb + (ks * 32 + g * 8 + (c16 >> 2)) * G_BST + (wc * 64 + j * 16 + (c16 & 3) * 4) * 2;
                bfr[j] = cat8(tr_read(p), tr_read(p + 4 * G_BST));
            }
#pragma unroll
            for (int i = 0; i < 4; ++i)
#pragma unroll
                for (int j = 0; j < 4; ++j) acc[i][j] = MFMA32(bfr[j], af[i], acc[i][j]);
        }
        if (kt + 1 < nk) swrite(buf ^ 1);
        __syncthreads();
    }
#pragma unroll
    for (int i = 0; i < 4; ++i) {
        const int m = m0 + wr * 64 + i * 16 + c16;
        if (EPI == 2) {
#pragma unroll
            for (int j = 0; j < 2; ++j) {
                float o[4];
#pragma unroll
                for (int r = 0; r < 4; ++r) {
                    const float gt = rbf(acc[i][j][r]), up = rbf(acc[i][j + 2][r]);
                    const float inner = 0.7978845608028654f * (gt + 0.044715f * gt * gt * gt);
                    const float ge = rbf(0.5f * gt * (1.0f + tanhf(inner)));
                    o[r] = ge * up;
                }
                v2u st = {pk2(o[0], o[1]), pk2(o[2], o[3])};
                *(v2u*)(C + (size_t)m * ldc + n0 + wc * 32 + j * 16 + 4 * g) = st;
            }
        } else {
#pragma unroll
            for (int j = 0; j < 4; ++j) {
                const int n = n0 + wc * 64 + j * 16 + 4 * g;
                float o[4];
#pragma unroll
                for (int r = 0; r < 4; ++r) o[r] = acc[i][j][r];
                if (EPI == 1) {
                    const v2u rr = *(const v2u*)(res + (size_t)m * ldc + n);
                    o[0] = __uint_as_float(rr[0] << 16) + rbf(o[0]); o[1] = __uint_as_float(rr[0] & 0xffff0000u) + rbf(o[1]);
                    o[2] = __uint_as_float(rr[1] << 16) + rbf(o[2]); o[3] = __uint_as_float(rr[1] & 0xffff0000u) + rbf(o[3]);
                }
                v2u st = {pk2(o[0], o[1]), pk2(o[2], o[3])};
                *(v2u*)(C + (size_t)m * ldc + n) = st;
            }
        }
    }
}

constexpr unsigned SENT = 0xFFFFFFFFu;
constexpr int C_WV = 0;
constexpr int C_MEM = 131072;
constexpr int C_QS = C_MEM + 8192;
constexpr int C_KS = C_QS + 8 * 272;
constexpr int C_VS = C_KS + 8 * 272;
constexpr int C_KX = C_VS + 2048;
constexpr int C_VX = C_KX + 32 * 272;
constexpr int C_LDS = C_VX + 32 * 288;
constexpr unsigned SPIN_CAP = 1u << 22;

__device__ __forceinline__ __amdgpu_buffer_rsrc_t mk_rsrc(const void* p, unsigned bytes) {
    return __builtin_amdgcn_make_buffer_rsrc((void*)p, 0, bytes, 0x00020000);
}
__device__ __forceinline__ v4u ld16_sc1(__amdgpu_buffer_rsrc_t r, unsigned off) { return __builtin_amdgcn_raw_buffer_load_b128(r, off, 0, 16); }
__device__ __forceinline__ void st16_sc1(v4u v, __amdgpu_buffer_rsrc_t r, unsigned off) { __builtin_amdgcn_raw_buffer_store_b128(v, r, off, 0, 16); }
__device__ __forceinline__ void st4_sc1(unsigned v, __amdgpu_buffer_rsrc_t r, unsigned off) { __builtin_amdgcn_raw_buffer_store_b32(v, r, off, 0, 16); }
__device__ __forceinline__ void dly(int n) { for (int i = 0; i < n; ++i) __builtin_amdgcn_s_sleep(1); }

__device__ __forceinline__ f32x4 comb2(f32x4 a, f32x4 b) {
    f32x4 o;
#pragma unroll
    for (int r = 0; r < 4; ++r) {
        auto sw = __builtin_amdgcn_permlane32_swap(__float_as_uint(a[r]), __float_as_uint(b[r]), false, false);
        o[r] = __uint_as_float(sw[0]) + __uint_as_float(sw[1]);
    }
    return o;
}

__global__ void __launch_bounds__(256, 1) k_chain(const u16* __restrict__ Wq, const u16* __restrict__ Wk, const u16* __restrict__ Wv,
                                                  const u16* __restrict__ Wo, const u16* __restrict__ mem0,
                                                  const u16* __restrict__ Kx, const u16* __restrict__ Vx,
                                                  u16* __restrict__ kmem, u16* __restrict__ vmem, unsigned char* __restrict__ ws_sync) {
    extern __shared__ __attribute__((aligned(16))) unsigned char smem[];
    LAS unsigned char* L = (LAS unsigned char*)smem;
    const int h = blockIdx.x, t = threadIdx.x, w = __builtin_amdgcn_readfirstlane(t >> 6), l = t & 63, g = l >> 4, c16 = l & 15;
    const auto rRS = mk_rsrc(ws_sync + WS_RS, 2u * 32 * 32 * 512);
    const auto rAG = mk_rsrc(ws_sync + WS_AG, 2u * 8192);
    const int sidx = (c16 < 8) ? 0x4444 : 0xEEEE;

    bf16x16 Bq[2][8], Bk[2][8], Bo[8][2];
    {
        constexpr int ST = C_MEM;
#pragma unroll
        for (int mat = 0; mat < 3; ++mat) {
            const u16* W = mat == 0 ? Wq : (mat == 1 ? Wk : Wv);
#pragma unroll
            for (int ks = 0; ks < 8; ++ks) {
                __syncthreads();
#pragma unroll
                for (int i = 0; i < 4; ++i) {
                    const int u = i * 256 + t, r = u >> 4, ch = u & 15;
                    *(LAS v4u*)(L + ST + r * 288 + ch * 16) = *(const v4u*)(W + (size_t)(64 * ks + r) * HD + h * DH + ch * 8);
                }
                __syncthreads();
#pragma unroll
                for (int tt = 0; tt < 2; ++tt) {
                    const LAS unsigned char* p = L + ST + (8 * g + (c16 >> 2)) * 288 + (32 * w + 16 * tt + (c16 & 3) * 4) * 2;
                    const bf16x8 lo = cat8(tr_read(p), tr_read(p + 4 * 288));
                    const bf16x8 hi = cat8(tr_read(p + 32 * 288), tr_read(p + 36 * 288));
                    if (mat == 0) Bq[tt][ks] = cat16(lo, hi);
                    else if (mat == 1) Bk[tt][ks] = cat16(lo, hi);
                    else {
                        *(LAS bf16x8*)(L + C_WV + (((2 * w + tt) * 8 + ks) * 2 + 0) * 1024 + l * 16) = lo;
                        *(LAS bf16x8*)(L + C_WV + (((2 * w + tt) * 8 + ks) * 2 + 1) * 1024 + l * 16) = hi;
                    }
                }
            }
        }
#pragma unroll
        for (int kk = 0; kk < 2; ++kk) {
#pragma unroll
            for (int cb = 0; cb < 4; ++cb) {
                __syncthreads();
#pragma unroll
                for (int i = 0; i < 4; ++i) {
                    const int u = i * 256 + t, k = u >> 4, ch = u & 15;
                    const int gg = k >> 4, wi = k & 15, ii = 2 * (wi >> 2) + (wi & 1), cc = (wi >> 1) & 1;
                    const int e = 16 * (4 * kk + 2 * cc + (ii >> 2)) + 4 * gg + (ii & 3);
                    *(LAS v4u*)(L + ST + k * 288 + ch * 16) = *(const v4u*)(Wo + ((size_t)h * DH + e) * DM + cb * 128 + ch * 8);
                }
                __syncthreads();
                if (w == cb) {
#pragma unroll
                    for (int tt = 0; tt < 8; ++tt) {
                        const LAS unsigned char* p = L + ST + (8 * g + (c16 >> 2)) * 288 + (16 * tt + (c16 & 3) * 4) * 2;
                        const bf16x8 lo = cat8(tr_read(p), tr_read(p + 4 * 288));
                        const bf16x8 hi = cat8(tr_read(p + 32 * 288), tr_read(p + 36 * 288));
                        Bo[tt][kk] = cat16(lo, hi);
                    }
                }
            }
        }
        __syncthreads();
    }
    {
#pragma unroll
        for (int i = 0; i < 2; ++i) {
            const int u = i * 256 + t, ks = u >> 6, ll = u & 63, gg = ll >> 4, cc = (ll >> 3) & 1, m = ll & 7;
            const u16* src = mem0 + m * DM + 64 * ks + 16 * gg + 2 * cc;
            v4u v;
#pragma unroll
            for (int q = 0; q < 4; ++q) v[q] = *(const unsigned*)(src + 4 * q);
            *(LAS v4u*)(L + C_MEM + u * 16) = v;
        }
#pragma unroll
        for (int i = 0; i < 2; ++i) {
            const int u = i * 256 + t, r = u >> 4, ch = u & 15;
            *(LAS v4u*)(L + C_KX + r * 272 + ch * 16) = *(const v4u*)(Kx + (size_t)r * HD + h * DH + ch * 8);
            *(LAS v4u*)(L + C_VX + r * 288 + ch * 16) = *(const v4u*)(Vx + (size_t)r * HD + h * DH + ch * 8);
        }
    }
    __syncthreads();

    for (int s = 0; s < NSEG; ++s) {
        v4u pk_[2], pv_[2];
        if (s + 1 < NSEG) {
#pragma unroll
            for (int i = 0; i < 2; ++i) {
                const int u = i * 256 + t, r = u >> 4, ch = u & 15;
                pk_[i] = *(const v4u*)(Kx + (size_t)((s + 1) * SEG + r) * HD + h * DH + ch * 8);
                pv_[i] = *(const v4u*)(Vx + (size_t)((s + 1) * SEG + r) * HD + h * DH + ch * 8);
            }
        }
        f32x4 aq[2], ak[2], av[2];
#pragma unroll
        for (int i = 0; i < 2; ++i) { aq[i] = (f32x4){0.f, 0.f, 0.f, 0.f}; ak[i] = aq[i]; av[i] = aq[i]; }
#pragma unroll
        for (int ks = 0; ks < 8; ++ks) {
            const bf16x8 a = *(const LAS bf16x8*)(L + C_MEM + ks * 1024 + l * 16);
#pragma unroll
            for (int tt = 0; tt < 2; ++tt) {
                const bf16x8 lo = *(const LAS bf16x8*)(L + C_WV + (((2 * w + tt) * 8 + ks) * 2 + 0) * 1024 + l * 16);
                const bf16x8 hi = *(const LAS bf16x8*)(L + C_WV + (((2 * w + tt) * 8 + ks) * 2 + 1) * 1024 + l * 16);
                aq[tt] = SMFMAC(a, Bq[tt][ks], aq[tt], sidx);
                ak[tt] = SMFMAC(a, Bk[tt][ks], ak[tt], sidx);
                av[tt] = SMFMAC(a, cat16(lo, hi), av[tt], sidx);
            }
        }
        {
            const f32x4 q = comb2(aq[0], aq[1]), k = comb2(ak[0], ak[1]), v = comb2(av[0], av[1]);
            const int e = 32 * w + 16 * (l >> 5) + c16, mb = 4 * (g & 1);
#pragma unroll
            for (int r = 0; r < 4; ++r) {
                *(LAS u16*)(L + C_QS + (mb + r) * 272 + e * 2) = f2bf(q[r]);
                *(LAS u16*)(L + C_KS + (mb + r) * 272 + e * 2) = f2bf(k[r]);
            }
            v2u vv = {pk2(v[0], v[1]), pk2(v[2], v[3])};
            *(LAS v2u*)(L + C_VS + e * 16 + mb * 2) = vv;
        }
        __syncthreads();
        if (t < 128) {
            const int m = t >> 4, ch = t & 15;
            *(v4u*)(kmem + ((size_t)(s * NH + h) * MM + m) * DH + ch * 8) = *(const LAS v4u*)(L + C_KS + m * 272 + ch * 16);
        } else {
            const int u = t - 128;
            *(v4u*)(vmem + (size_t)(s * NH + h) * (MM * DH) + u * 8) = *(const LAS v4u*)(L + C_VS + u * 16);
        }
        if (s == NSEG - 1) break;
        bf16x8 aO[2];
        {
            f32x4 sc[3];
#pragma unroll
            for (int T = 0; T < 3; ++T) sc[T] = (f32x4){0.f, 0.f, 0.f, 0.f};
#pragma unroll
            for (int kq = 0; kq < 4; ++kq) {
                const bf16x8 qb = *(const LAS bf16x8*)(L + C_QS + (c16 & 7) * 272 + (32 * kq + 8 * g) * 2);
                const bf16x8 k0 = *(const LAS bf16x8*)(L + C_KX + c16 * 272 + (32 * kq + 8 * g) * 2);
                const bf16x8 k1 = *(const LAS bf16x8*)(L + C_KX + (16 + c16) * 272 + (32 * kq + 8 * g) * 2);
                const bf16x8 k2 = *(const LAS bf16x8*)(L + C_KS + (c16 & 7) * 272 + (32 * kq + 8 * g) * 2);
                sc[0] = MFMA32(k0, qb, sc[0]); sc[1] = MFMA32(k1, qb, sc[1]); sc[2] = MFMA32(k2, qb, sc[2]);
            }
            const float scale = 0.08838834764831845f;
            float p[12];
            float mx = -3.0e38f;
#pragma unroll
            for (int T = 0; T < 3; ++T)
#pragma unroll
                for (int r = 0; r < 4; ++r) {
                    float v = rbf(rbf(sc[T][r]) * scale);
                    if (T == 2 && g >= 2) v = -3.0e38f;
                    p[T * 4 + r] = v; mx = fmaxf(mx, v);
                }
            mx = fmaxf(mx, __shfl_xor(mx, 16)); mx = fmaxf(mx, __shfl_xor(mx, 32));
            float sum = 0.f;
#pragma unroll
            for (int i = 0; i < 12; ++i) { p[i] = (i >= 8 && g >= 2) ? 0.f : __expf(p[i] - mx); sum += p[i]; }
            sum += __shfl_xor(sum, 16); sum += __shfl_xor(sum, 32);
            const float inv = 1.0f / sum;
            v4u px = {pk2(p[0] * inv, p[1] * inv), pk2(p[2] * inv, p[3] * inv), pk2(p[4] * inv, p[5] * inv), pk2(p[6] * inv, p[7] * inv)};
            v2u pm = {pk2(p[8] * inv, p[9] * inv), pk2(p[10] * inv, p[11] * inv)};
            const bf16x8 Px = __builtin_bit_cast(bf16x8, px);
            const bf16x8 Pm = __builtin_bit_cast(bf16x8, (v4u){pm[0], pm[1], 0u, 0u});
            unsigned ob[8][2];
#pragma unroll
            for (int T = 0; T < 8; ++T) {
                const LAS unsigned char* pv = L + C_VX + (4 * g + (c16 >> 2)) * 288 + (16 * T + (c16 & 3) * 4) * 2;
                const bf16x8 va = cat8(tr_read(pv), tr_read(pv + 16 * 288));
                v2u vm = *(const LAS v2u*)(L + C_VS + (16 * T + c16) * 16 + (g & 1) * 8);
                if (g >= 2) vm = (v2u){0u, 0u};
                f32x4 o = (f32x4){0.f, 0.f, 0.f, 0.f};
                o = MFMA32(va, Px, o);
                o = MFMA32(__builtin_bit_cast(bf16x8, (v4u){vm[0], vm[1], 0u, 0u}), Pm, o);
                ob[T][0] = pk2(o[0], o[1]); ob[T][1] = pk2(o[2], o[3]);
            }
            const bool hiC = c16 >= 8;
#pragma unroll
            for (int kk = 0; kk < 2; ++kk) {
                v4u a;
                a[0] = hiC ? ob[4 * kk + 2][0] : ob[4 * kk][0]; a[1] = hiC ? ob[4 * kk + 2][1] : ob[4 * kk][1];
                a[2] = hiC ? ob[4 * kk + 3][0] : ob[4 * kk + 1][0]; a[3] = hiC ? ob[4 * kk + 3][1] : ob[4 * kk + 1][1];
                aO[kk] = __builtin_bit_cast(bf16x8, a);
            }
        }
        const unsigned slot = s & 1, par = (s >> 1) & 1;
#pragma unroll
        for (int tp = 0; tp < 4; ++tp) {
            f32x4 o0 = (f32x4){0.f, 0.f, 0.f, 0.f}, o1 = o0;
            o0 = SMFMAC(aO[0], Bo[2 * tp][0], o0, sidx); o0 = SMFMAC(aO[1], Bo[2 * tp][1], o0, sidx);
            o1 = SMFMAC(aO[0], Bo[2 * tp + 1][0], o1, sidx); o1 = SMFMAC(aO[1], Bo[2 * tp + 1][1], o1, sidx);
            const f32x4 o = comb2(o0, o1);
            v4u st;
#pragma unroll
            for (int r = 0; r < 4; ++r) st[r] = (__float_as_uint(o[r]) & ~1u) | par;
            const unsigned cons = 8 * w + 2 * tp + (l >> 5);
            st16_sc1(st, rRS, slot * 524288u + cons * 16384u + h * 512u + (l & 31) * 16u);
        }
        dly(14);
        {
            v4u x[4];
            unsigned spins = 0;
            const unsigned base = slot * 524288u + h * 16384u + w * 4096u + l * 16u;
            for (;;) {
                asm volatile("" ::: "memory");
                unsigned bad = 0;
#pragma unroll
                for (int i = 0; i < 4; ++i) x[i] = ld16_sc1(rRS, base + i * 1024u);
#pragma unroll
                for (int i = 0; i < 4; ++i) bad |= (x[i][0] ^ par) | (x[i][1] ^ par) | (x[i][2] ^ par) | (x[i][3] ^ par);
                if (__all((bad & 1u) == 0)) break;
                if (++spins > SPIN_CAP) break;
            }
            float sm[4];
#pragma unroll
            for (int r = 0; r < 4; ++r) {
                float v = (__uint_as_float(x[0][r]) + __uint_as_float(x[1][r])) + (__uint_as_float(x[2][r]) + __uint_as_float(x[3][r]));
                v += __shfl_xor(v, 32);
                sm[r] = v;
            }
            if (l < 32) {
#pragma unroll
                for (int r = 0; r < 4; ++r) *(LAS float*)(L + C_MEM + w * 512 + ((4 * g + r) * 16 + c16) * 4) = sm[r];
            }
        }
        __syncthreads();
        if (w == 0) {
            const int m = l >> 3, cp = l & 7;
            float a = 0.f, b = 0.f;
#pragma unroll
            for (int ww = 0; ww < 4; ++ww) {
                const v2u v = *(const LAS v2u*)(L + C_MEM + ww * 512 + (m * 16 + 2 * cp) * 4);
                a += __uint_as_float(v[0]); b += __uint_as_float(v[1]);
            }
            unsigned pkd = pk2(a, b);
            if (pkd == SENT) pkd = 0x7FC07FC0u;
            const unsigned off = (h >> 2) * 1024u + ((h & 3) * 16 + (cp & 1) * 8 + m) * 16u + (cp >> 1) * 4u;
            st4_sc1(pkd, rAG, slot * 8192u + off);
            st4_sc1(SENT, rAG, (slot ^ 1) * 8192u + off);
        }
#pragma unroll
        for (int i = 0; i < 2; ++i) {
            const int u = i * 256 + t, r = u >> 4, ch = u & 15;
            *(LAS v4u*)(L + C_KX + r * 272 + ch * 16) = pk_[i];
            *(LAS v4u*)(L + C_VX + r * 288 + ch * 16) = pv_[i];
        }
        __syncthreads();
        dly(12);
        {
            v4u x[2];
            unsigned spins = 0;
            for (;;) {
                asm volatile("" ::: "memory");
                bool ok = true;
#pragma unroll
                for (int i = 0; i < 2; ++i) x[i] = ld16_sc1(rAG, slot * 8192u + (i * 256 + t) * 16u);
#pragma unroll
                for (int i = 0; i < 2; ++i) ok &= (x[i][0] != SENT) & (x[i][1] != SENT) & (x[i][2] != SENT) & (x[i][3] != SENT);
                if (__all(ok)) break;
                if (++spins > SPIN_CAP) break;
            }
#pragma unroll
            for (int i = 0; i < 2; ++i) *(LAS v4u*)(L + C_MEM + (i * 256 + t) * 16) = x[i];
        }
        __syncthreads();
    }
}

__global__ void __launch_bounds__(256) k_xattn(const u16* __restrict__ Q, const u16* __restrict__ Kx, const u16* __restrict__ Vx,
                                               const u16* __restrict__ kmem, const u16* __restrict__ vmem, u16* __restrict__ O) {
    __shared__ __attribute__((aligned(16))) unsigned char smem[4 * 32 * 288];
    const int t = threadIdx.x, w = t >> 6, l = t & 63, g = l >> 4, c16 = l & 15;
    const int task = blockIdx.x * 4 + w, s = task >> 5, h = task & 31;
    LAS unsigned char* LV = (LAS unsigned char*)smem + w * 32 * 288;
    const size_t rowb = (size_t)s * SEG;
#pragma unroll
    for (int i = 0; i < 8; ++i) {
        const int u = i * 64 + l, r = u >> 4, ch = u & 15;
        *(LAS v4u*)(LV + r * 288 + ch * 16) = *(const v4u*)(Vx + (rowb + r) * HD + h * DH + ch * 8);
    }
    f32x4 sc[2][3];
#pragma unroll
    for (int qt = 0; qt < 2; ++qt)
#pragma unroll
        for (int T = 0; T < 3; ++T) sc[qt][T] = (f32x4){0.f, 0.f, 0.f, 0.f};
#pragma unroll
    for (int kq = 0; kq < 4; ++kq) {
        const int eo = 32 * kq + 8 * g;
        const bf16x8 q0 = *(const bf16x8*)(Q + (rowb + c16) * HD + h * DH + eo);
        const bf16x8 q1 = *(const bf16x8*)(Q + (rowb + 16 + c16) * HD + h * DH + eo);
        const bf16x8 k0 = *(const bf16x8*)(Kx + (rowb + c16) * HD + h * DH + eo);
        const bf16x8 k1 = *(const bf16x8*)(Kx + (rowb + 16 + c16) * HD + h * DH + eo);
        const bf16x8 k2 = *(const bf16x8*)(kmem + ((size_t)(s * NH + h) * MM + (c16 & 7)) * DH + eo);
        sc[0][0] = MFMA32(k0, q0, sc[0][0]); sc[0][1] = MFMA32(k1, q0, sc[0][1]); sc[0][2] = MFMA32(k2, q0, sc[0][2]);
        sc[1][0] = MFMA32(k0, q1, sc[1][0]); sc[1][1] = MFMA32(k1, q1, sc[1][1]); sc[1][2] = MFMA32(k2, q1, sc[1][2]);
    }
    bf16x8 Px[2], Pm[2];
    const float scale = 0.08838834764831845f;
#pragma unroll
    for (int qt = 0; qt < 2; ++qt) {
        const int qi = 16 * qt + c16;
        float p[12]; float mx = -3.0e38f;
#pragma unroll
        for (int T = 0; T < 3; ++T)
#pragma unroll
            for (int r = 0; r < 4; ++r) {
                float v = rbf(rbf(sc[qt][T][r]) * scale);
                const bool valid = (T == 2) ? (g < 2) : (16 * T + 4 * g + r <= qi);
                if (!valid) v = -3.0e38f;
                p[T * 4 + r] = v; mx = fmaxf(mx, v);
            }
        mx = fmaxf(mx, __shfl_xor(mx, 16)); mx = fmaxf(mx, __shfl_xor(mx, 32));
        float sum = 0.f;
#pragma unroll
        for (int i = 0; i < 12; ++i) { p[i] = (p[i] < -1.0e38f) ? 0.f : __expf(p[i] - mx); sum += p[i]; }
        sum += __shfl_xor(sum, 16); sum += __shfl_xor(sum, 32);
        const float inv = 1.0f / sum;
        v4u px = {pk2(p[0] * inv, p[1] * inv), pk2(p[2] * inv, p[3] * inv), pk2(p[4] * inv, p[5] * inv), pk2(p[6] * inv, p[7] * inv)};
        v2u pm = {pk2(p[8] * inv, p[9] * inv), pk2(p[10] * inv, p[11] * inv)};
        Px[qt] = __builtin_bit_cast(bf16x8, px); Pm[qt] = __builtin_bit_cast(bf16x8, (v4u){pm[0], pm[1], 0u, 0u});
    }
    __syncthreads();
#pragma unroll
    for (int T = 0; T < 8; ++T) {
        const LAS unsigned char* pv = LV + (4 * g + (c16 >> 2)) * 288 + (16 * T + (c16 & 3) * 4) * 2;
        const bf16x8 va = cat8(tr_read(pv), tr_read(pv + 16 * 288));
        v2u vm = *(const v2u*)(vmem + (size_t)(s * NH + h) * (MM * DH) + (16 * T + c16) * 8 + (g & 1) * 4);
        if (g >= 2) vm = (v2u){0u, 0u};
#pragma unroll
        for (int qt = 0; qt < 2; ++qt) {
            f32x4 o = (f32x4){0.f, 0.f, 0.f, 0.f};
            o = MFMA32(va, Px[qt], o);
            o = MFMA32(__builtin_bit_cast(bf16x8, (v4u){vm[0], vm[1], 0u, 0u}), Pm[qt], o);
            v2u st = {pk2(o[0], o[1]), pk2(o[2], o[3])};
            *(v2u*)(O + (rowb + 16 * qt + c16) * HD + h * DH + 16 * T + 4 * g) = st;
        }
    }
}

extern "C" void kernel_launch(void* const* d_in, const int* in_sizes, int n_in, void* d_out, int out_size, void* d_ws, size_t ws_size,
                              hipStream_t stream) {
    const u16* x = (const u16*)d_in[0];
    const u16* mem0 = (const u16*)d_in[1];
    const u16* Wq = (const u16*)d_in[2];
    const u16* Wk = (const u16*)d_in[3];
    const u16* Wv = (const u16*)d_in[4];
    const u16* Wo = (const u16*)d_in[5];
    const u16* wg = (const u16*)d_in[8];
    const u16* wu = (const u16*)d_in[9];
    const u16* wd = (const u16*)d_in[10];
    u16* out = (u16*)d_out;
    unsigned char* ws = (unsigned char*)d_ws;
    u16* xn = (u16*)(ws + WS_XN); u16* Q = (u16*)(ws + WS_Q); u16* K = (u16*)(ws + WS_K); u16* V = (u16*)(ws + WS_V);
    u16* O = (u16*)(ws + WS_O); u16* km = (u16*)(ws + WS_KM); u16* vm = (u16*)(ws + WS_VM);
    u16* H = (u16*)(ws + WS_H); u16* HN = (u16*)(ws + WS_HN); u16* ACT = (u16*)(ws + WS_ACT);

    (void)hipFuncSetAttribute((const void*)k_chain, hipFuncAttributeMaxDynamicSharedMemorySize, C_LDS);
    (void)hipFuncSetAttribute((const void*)k_gemm<0>, hipFuncAttributeMaxDynamicSharedMemorySize, 2 * G_STAGE);
    (void)hipFuncSetAttribute((const void*)k_gemm<1>, hipFuncAttributeMaxDynamicSharedMemorySize, 2 * G_STAGE);
    (void)hipFuncSetAttribute((const void*)k_gemm<2>, hipFuncAttributeMaxDynamicSharedMemorySize, 2 * G_STAGE);
    (void)hipMemsetAsync(ws, 0xFF, WS_SYNC_END, stream);
    k_ln<<<S_LEN / 4, 256, 0, stream>>>(x, xn);
    const int gsm = 2 * G_STAGE;
    k_gemm<0><<<dim3(HD / G_BN, S_LEN / G_BM), 256, gsm, stream>>>(xn, DM, Wq, nullptr, HD, Q, HD, nullptr, DM);
    k_gemm<0><<<dim3(HD / G_BN, S_LEN / G_BM), 256, gsm, stream>>>(xn, DM, Wk, nullptr, HD, K, HD, nullptr, DM);
    k_gemm<0><<<dim3(HD / G_BN, S_LEN / G_BM), 256, gsm, stream>>>(xn, DM, Wv, nullptr, HD, V, HD, nullptr, DM);
    k_chain<<<NH, 256, C_LDS, stream>>>(Wq, Wk, Wv, Wo, mem0, K, V, km, vm, ws);
    k_xattn<<<NSEG * NH / 4, 256, 0, stream>>>(Q, K, V, km, vm, O);
    k_gemm<1><<<dim3(DM / G_BN, S_LEN / G_BM), 256, gsm, stream>>>(O, HD, Wo, nullptr, DM, H, DM, x, HD);
    k_ln<<<S_LEN / 4, 256, 0, stream>>>(H, HN);
    k_gemm<2><<<dim3(FF / 64, S_LEN / G_BM), 256, gsm, stream>>>(HN, DM, wg, wu, FF, ACT, FF, nullptr, DM);
    k_gemm<1><<<dim3(DM / G_BN, S_LEN / G_BM), 256, gsm, stream>>>(ACT, FF, wd, nullptr, DM, out, DM, H, FF);
}
```

```cpp
#include <hip/hip_runtime.h>
#include <hip/hip_bf16.h>
#include <stdint.h>

typedef unsigned short u16;
typedef __bf16 bf16x8 __attribute__((ext_vector_type(8)));
typedef __bf16 bf16x4 __attribute__((ext_vector_type(4)));
typedef __bf16 bf16x16 __attribute__((ext_vector_type(16)));
typedef short s16x4 __attribute__((ext_vector_type(4)));
typedef float f32x4 __attribute__((ext_vector_type(4)));
typedef unsigned v4u __attribute__((ext_vector_type(4)));
typedef unsigned v2u __attribute__((ext_vector_type(2)));
#define LAS __attribute__((address_space(3)))

constexpr int S_LEN = 2048, DM = 512, NH = 32, DH = 128, HD = NH * DH, MM = 8, SEG = 32, NSEG = S_LEN / SEG, FF = 1792;

constexpr size_t WS_RS = 0;
constexpr size_t WS_AG = WS_RS + 2u * 32 * 32 * 512;
constexpr size_t WS_SYNC_END = WS_AG + 2u * 8192;
constexpr size_t WS_FLAGS = WS_SYNC_END;
constexpr int FL_QKV = 0;
constexpr int FL_PROG = 16 * 32 * 16;
constexpr int FL_WORDS = FL_PROG + 64;
constexpr size_t WS_FLAGS_END = WS_FLAGS + FL_WORDS * 4;
constexpr size_t WS_XN = 2u << 20;
constexpr size_t WS_Q = WS_XN + (size_t)S_LEN * DM * 2;
constexpr size_t WS_K = WS_Q + (size_t)S_LEN * HD * 2;
constexpr size_t WS_V = WS_K + (size_t)S_LEN * HD * 2;
constexpr size_t WS_O = WS_V + (size_t)S_LEN * HD * 2;
constexpr size_t WS_KM = WS_O + (size_t)S_LEN * HD * 2;
constexpr size_t WS_VM = WS_KM + (size_t)NSEG * NH * MM * DH * 2;
constexpr size_t WS_H = WS_VM + (size_t)NSEG * NH * MM * DH * 2;
constexpr size_t WS_HN = WS_H + (size_t)S_LEN * DM * 2;
constexpr size_t WS_ACT = WS_HN + (size_t)S_LEN * DM * 2;
constexpr size_t WS_END = WS_ACT + (size_t)S_LEN * FF * 2;

__device__ __forceinline__ float bf2f(u16 v) { return __uint_as_float((unsigned)v << 16); }
__device__ __forceinline__ u16 f2bf(float f) { return __builtin_bit_cast(u16, (__bf16)f); }
__device__ __forceinline__ float rbf(float f) { return (float)(__bf16)f; }
typedef __bf16 bf16x2 __attribute__((ext_vector_type(2)));
typedef float f32x2 __attribute__((ext_vector_type(2)));
__device__ __forceinline__ unsigned pk2(float lo, float hi) {
    const f32x2 v = {lo, hi};
    return __builtin_bit_cast(unsigned, __builtin_convertvector(v, bf16x2));
}
__device__ __forceinline__ float xl32_add(float v) { auto r = __builtin_amdgcn_permlane32_swap(__float_as_uint(v), __float_as_uint(v), false, false); return __uint_as_float(r[0]) + __uint_as_float(r[1]); }
__device__ __forceinline__ float xl16_add(float v) { auto r = __builtin_amdgcn_permlane16_swap(__float_as_uint(v), __float_as_uint(v), false, false); return __uint_as_float(r[0]) + __uint_as_float(r[1]); }
__device__ __forceinline__ float xl32_max(float v) { auto r = __builtin_amdgcn_permlane32_swap(__float_as_uint(v), __float_as_uint(v), false, false); return fmaxf(__uint_as_float(r[0]), __uint_as_float(r[1])); }
__device__ __forceinline__ float xl16_max(float v) { auto r = __builtin_amdgcn_permlane16_swap(__float_as_uint(v), __float_as_uint(v), false, false); return fmaxf(__uint_as_float(r[0]), __uint_as_float(r[1])); }
__device__ __forceinline__ float xl8_add(float v) { return v + __uint_as_float(__builtin_amdgcn_update_dpp(0, __float_as_uint(v), 0x128, 0xf, 0xf, false)); }
__device__ __forceinline__ float xl1_get(float v) { return __uint_as_float(__builtin_amdgcn_update_dpp(0, __float_as_uint(v), 0xB1, 0xf, 0xf, false)); }
#define SBAR() __builtin_amdgcn_sched_barrier(0)
__device__ __forceinline__ s16x4 tr_read(const LAS unsigned char* p) {
    return __builtin_amdgcn_ds_read_tr16_b64_v4i16((LAS s16x4*)p);
}
__device__ __forceinline__ bf16x8 cat8(s16x4 a, s16x4 b) {
    typedef short s16x8 __attribute__((ext_vector_type(8)));
    s16x8 r = __builtin_shufflevector(a, b, 0, 1, 2, 3, 4, 5, 6, 7);
    return __builtin_bit_cast(bf16x8, r);
}
__device__ __forceinline__ bf16x16 cat16(bf16x8 a, bf16x8 b) {
    return __builtin_shufflevector(a, b, 0, 1, 2, 3, 4, 5, 6, 7, 8, 9, 10, 11, 12, 13, 14, 15);
}
#define MFMA32(a, b, c) __builtin_amdgcn_mfma_f32_16x16x32_bf16((a), (b), (c), 0, 0, 0)
#define SMFMAC(a, b, c, idx) __builtin_amdgcn_smfmac_f32_16x16x64_bf16((a), (b), (c), (idx), 0, 0)

template <bool INIT>
__global__ void __launch_bounds__(256) k_ln(const u16* __restrict__ x, u16* __restrict__ y, unsigned char* __restrict__ ws) {
    const int row = blockIdx.x * 4 + (threadIdx.x >> 6), lane = threadIdx.x & 63;
    if (INIT) {
        const size_t u = (size_t)blockIdx.x * 256 + threadIdx.x;
        if (u * 16 < WS_SYNC_END) *(v4u*)(ws + u * 16) = (v4u){0xFFFFFFFFu, 0xFFFFFFFFu, 0xFFFFFFFFu, 0xFFFFFFFFu};
        else if (u * 16 < WS_FLAGS_END) *(v4u*)(ws + u * 16) = (v4u){0u, 0u, 0u, 0u};
    }
    const v4u raw = *(const v4u*)(x + (size_t)row * DM + lane * 8);
    float v[8];
#pragma unroll
    for (int i = 0; i < 4; ++i) { v[2 * i] = __uint_as_float(raw[i] << 16); v[2 * i + 1] = __uint_as_float(raw[i] & 0xffff0000u); }
    float s = 0.f;
#pragma unroll
    for (int i = 0; i < 8; ++i) s += v[i];
#pragma unroll
    for (int o = 32; o >= 1; o >>= 1) s += __shfl_xor(s, o);
    const float mean = s * (1.0f / DM);
    float q = 0.f;
#pragma unroll
    for (int i = 0; i < 8; ++i) { const float d = v[i] - mean; q += d * d; }
#pragma unroll
    for (int o = 32; o >= 1; o >>= 1) q += __shfl_xor(q, o);
    const float mb = rbf(mean), vb = rbf(q * (1.0f / DM));
    const float den = rbf(sqrtf(rbf(vb + 1e-5f)));
    v4u o;
#pragma unroll
    for (int i = 0; i < 4; ++i) o[i] = pk2(rbf(v[2 * i] - mb) / den, rbf(v[2 * i + 1] - mb) / den);
    *(v4u*)(y + (size_t)row * DM + lane * 8) = o;
}

constexpr int G_BK = 64, G_AST = 144;
__host__ __device__ constexpr int g_stage(int BM, int BN) { return BM * G_AST + G_BK * (BN * 2 + 32); }
__device__ __forceinline__ void st8_dev(u16* p, v2u v) {
    __hip_atomic_store((unsigned long long*)p, (unsigned long long)v[0] | ((unsigned long long)v[1] << 32), __ATOMIC_RELAXED, __HIP_MEMORY_SCOPE_AGENT);
}
template <int EPI, bool DEV, int BM, int BN>
__device__ __forceinline__ void gemm_tile(unsigned char* smem, const u16* __restrict__ A, int lda, const u16* __restrict__ B, const u16* __restrict__ B2, int ldb,
                                          u16* __restrict__ C, int ldc, const u16* __restrict__ res, int K, int m0, int n0) {
    constexpr int BST = BN * 2 + 32, ABYTES = BM * G_AST, STAGE = g_stage(BM, BN);
    constexpr int NA = BM / 32, NB = BN / 32, MI = BM / 32, NJ = BN / 32, BCH = BN / 8;
    const int t = threadIdx.x, w = t >> 6, l = t & 63, g = l >> 4, c16 = l & 15;
    const int wr = w >> 1, wc = w & 1;
    v4u ra[2][NA], rb[2][NB];
    auto gload = [&](int k0, v4u (&da)[NA], v4u (&db)[NB]) {
#pragma unroll
        for (int i = 0; i < NA; ++i) { const int u = i * 256 + t; da[i] = *(const v4u*)(A + (size_t)(m0 + (u >> 3)) * lda + k0 + (u & 7) * 8); }
#pragma unroll
        for (int i = 0; i < NB; ++i) {
            const int u = i * 256 + t, kr = u / BCH, ch = u % BCH;
            if (EPI == 2) {
                constexpr int Q = BCH / 4;
                const int wcc = ch / (2 * Q), part = (ch / Q) & 1, cc = (ch % Q) * 8;
                db[i] = *(const v4u*)((part ? B2 : B) + (size_t)(k0 + kr) * ldb + n0 + (BN / 4) * wcc + cc);
            } else db[i] = *(const v4u*)(B + (size_t)(k0 + kr) * ldb + n0 + ch * 8);
        }
    };
    auto swrite = [&](int buf, v4u (&da)[NA], v4u (&db)[NB]) {
        unsigned char* sa = smem + buf * STAGE; unsigned char* sb = sa + ABYTES;
#pragma unroll
        for (int i = 0; i < NA; ++i) { const int u = i * 256 + t; *(v4u*)(sa + (u >> 3) * G_AST + (u & 7) * 16) = da[i]; }
#pragma unroll
        for (int i = 0; i < NB; ++i) { const int u = i * 256 + t; *(v4u*)(sb + (u / BCH) * BST + (u % BCH) * 16) = db[i]; }
    };
    f32x4 acc[MI][NJ];
#pragma unroll
    for (int i = 0; i < MI; ++i)
#pragma unroll
        for (int j = 0; j < NJ; ++j) acc[i][j] = (f32x4){0.f, 0.f, 0.f, 0.f};
    auto compute = [&](int buf) {
        const LAS unsigned char* sa = (const LAS unsigned char*)smem + buf * STAGE;
        const LAS unsigned char* sb = sa + ABYTES;
#pragma unroll
        for (int ks = 0; ks < 2; ++ks) {
            bf16x8 af[MI], bfr[NJ];
#pragma unroll
            for (int i = 0; i < MI; ++i) af[i] = *(const LAS bf16x8*)(sa + (wr * (BM / 2) + i * 16 + c16) * G_AST + (ks * 32 + g * 8) * 2);
#pragma unroll
            for (int j = 0; j < NJ; ++j) {
                const LAS unsigned char* p = sb + (ks * 32 + g * 8 + (c16 >> 2)) * BST + (wc * (BN / 2) + j * 16 + (c16 & 3) * 4) * 2;
                bfr[j] = cat8(tr_read(p), tr_read(p + 4 * BST));
            }
#pragma unroll
            for (int i = 0; i < MI; ++i)
#pragma unroll
                for (int j = 0; j < NJ; ++j) acc[i][j] = MFMA32(bfr[j], af[i], acc[i][j]);
        }
    };
    const int nk = K / G_BK;
    gload(0, ra[0], rb[0]); gload(G_BK, ra[1], rb[1]);
    __syncthreads();
    swrite(0, ra[0], rb[0]);
    if (2 < nk) gload(2 * G_BK, ra[0], rb[0]);
    __syncthreads();
    for (int kt = 0; kt < nk; kt += 2) {
        swrite(1, ra[1], rb[1]);
        if (kt + 3 < nk) gload((kt + 3) * G_BK, ra[1], rb[1]);
        compute(0);
        __syncthreads();
        if (kt + 2 < nk) { swrite(0, ra[0], rb[0]); if (kt + 4 < nk) gload((kt + 4) * G_BK, ra[0], rb[0]); }
        compute(1);
        __syncthreads();
    }
#pragma unroll
    for (int i = 0; i < MI; ++i) {
        const int m = m0 + wr * (BM / 2) + i * 16 + c16;
        if (EPI == 2) {
#pragma unroll
            for (int j = 0; j < NJ / 2; ++j) {
                float o[4];
#pragma unroll
                for (int r = 0; r < 4; ++r) {
                    const float gt = rbf(acc[i][j][r]), up = rbf(acc[i][j + NJ / 2][r]);
                    const float inner = 0.7978845608028654f * (gt + 0.044715f * gt * gt * gt);
                    const float ge = rbf(0.5f * gt * (1.0f + tanhf(inner)));
                    o[r] = ge * up;
                }
                v2u st = {pk2(o[0], o[1]), pk2(o[2], o[3])};
                u16* dst = C + (size_t)m * ldc + n0 + wc * (BN / 4) + j * 16 + 4 * g;
                if (DEV) st8_dev(dst, st); else *(v2u*)dst = st;
            }
        } else {
#pragma unroll
            for (int j = 0; j < NJ; ++j) {
                const int n = n0 + wc * (BN / 2) + j * 16 + 4 * g;
                float o[4];
#pragma unroll
                for (int r = 0; r < 4; ++r) o[r] = acc[i][j][r];
                if (EPI == 1) {
                    const v2u rr = *(const v2u*)(res + (size_t)m * ldc + n);
                    o[0] = __uint_as_float(rr[0] << 16) + rbf(o[0]); o[1] = __uint_as_float(rr[0] & 0xffff0000u) + rbf(o[1]);
                    o[2] = __uint_as_float(rr[1] << 16) + rbf(o[2]); o[3] = __uint_as_float(rr[1] & 0xffff0000u) + rbf(o[3]);
                }
                v2u st = {pk2(o[0], o[1]), pk2(o[2], o[3])};
                if (DEV) st8_dev(C + (size_t)m * ldc + n, st); else *(v2u*)(C + (size_t)m * ldc + n) = st;
            }
        }
    }
}
template <int EPI, int BM, int BN>
__global__ void __launch_bounds__(256) k_gemm(const u16* __restrict__ A, int lda, const u16* __restrict__ B, const u16* __restrict__ B2, int ldb,
                                              u16* __restrict__ C, int ldc, const u16* __restrict__ res, int K) {
    extern __shared__ __attribute__((aligned(16))) unsigned char smem[];
    gemm_tile<EPI, false, BM, BN>(smem, A, lda, B, B2, ldb, C, ldc, res, K, blockIdx.y * BM, blockIdx.x * (EPI == 2 ? BN / 2 : BN));
}

constexpr unsigned SENT = 0xFFFFFFFFu;
constexpr int C_WV = 0;
constexpr int C_MEM = 131072;
constexpr int RST = 544;
constexpr int C_QS = C_MEM + 8192;
constexpr int C_KS = C_QS + 256;
constexpr int C_VS = C_QS + 8 * RST;
constexpr int C_KX = C_VS + 2048;
constexpr int C_VX = C_KX + 256;
constexpr int C_LDS = C_KX + 32 * RST;
constexpr unsigned SPIN_CAP = 1u << 22;
#define DLY1 14
#define DLY2 12

__device__ __forceinline__ __amdgpu_buffer_rsrc_t mk_rsrc(const void* p, unsigned bytes) {
    return __builtin_amdgcn_make_buffer_rsrc((void*)p, 0, bytes, 0x00020000);
}
__device__ __forceinline__ v4u ld16_sc1(__amdgpu_buffer_rsrc_t r, unsigned off) { return __builtin_amdgcn_raw_buffer_load_b128(r, off, 0, 16); }
__device__ __forceinline__ void st16_sc1(v4u v, __amdgpu_buffer_rsrc_t r, unsigned off) { __builtin_amdgcn_raw_buffer_store_b128(v, r, off, 0, 16); }
__device__ __forceinline__ void st4_sc1(unsigned v, __amdgpu_buffer_rsrc_t r, unsigned off) { __builtin_amdgcn_raw_buffer_store_b32(v, r, off, 0, 16); }
__device__ __forceinline__ void dly(int n) { for (int i = 0; i < n; ++i) __builtin_amdgcn_s_sleep(1); }

__device__ __forceinline__ f32x4 comb2(f32x4 a, f32x4 b) {
    f32x4 o;
#pragma unroll
    for (int r = 0; r < 4; ++r) {
        auto sw = __builtin_amdgcn_permlane32_swap(__float_as_uint(a[r]), __float_as_uint(b[r]), false, false);
        o[r] = __uint_as_float(sw[0]) + __uint_as_float(sw[1]);
    }
    return o;
}

#define STAMP(i)
__device__ __forceinline__ unsigned ld_flag(const unsigned* p) { return __hip_atomic_load(p, __ATOMIC_RELAXED, __HIP_MEMORY_SCOPE_AGENT); }
__device__ __forceinline__ void wait_flag_ge(const unsigned* p, unsigned want, int sleep_n) {
    unsigned spins = 0;
    while ((unsigned)__builtin_amdgcn_readfirstlane(ld_flag(p)) < want) { dly(sleep_n); if (++spins > SPIN_CAP) break; }
}
__device__ __forceinline__ void wait_flag_kv(const unsigned* p) {
    unsigned spins = 0;
    while (((unsigned)__builtin_amdgcn_readfirstlane(ld_flag(p)) & 3u) != 2u) { dly(2); if (++spins > SPIN_CAP) break; }
}
__device__ __forceinline__ void chain_body(unsigned char* smem, const u16* __restrict__ Wq, const u16* __restrict__ Wk, const u16* __restrict__ Wv,
                                           const u16* __restrict__ Wo, const u16* __restrict__ mem0,
                                           const u16* Kx, const u16* Vx, u16* kmem, u16* vmem, unsigned char* ws_sync) {
    LAS unsigned char* L = (LAS unsigned char*)smem;
    unsigned* flags = (unsigned*)(ws_sync + WS_FLAGS);
    const auto rK = mk_rsrc(Kx, (unsigned)S_LEN * HD * 2);
    const auto rV = mk_rsrc(Vx, (unsigned)S_LEN * HD * 2);
    const auto rKM = mk_rsrc(kmem, (unsigned)NSEG * NH * MM * DH * 2);
    const auto rVM = mk_rsrc(vmem, (unsigned)NSEG * NH * MM * DH * 2);
    const int h = blockIdx.x, t = threadIdx.x, w = __builtin_amdgcn_readfirstlane(t >> 6), l = t & 63, g = l >> 4, c16 = l & 15;
    const auto rRS = mk_rsrc(ws_sync + WS_RS, 2u * 32 * 32 * 512);
    const auto rAG = mk_rsrc(ws_sync + WS_AG, 2u * 8192);
    const int sidx = (c16 < 8) ? 0x4444 : 0xEEEE;

    bf16x16 Bq[2][8], Bk[2][8], Bo[8][2];
    {
        constexpr int ST = C_MEM;
        v4u P[4][4];
        auto gl = [&](int r, v4u (&d)[4]) {
#pragma unroll
            for (int i = 0; i < 4; ++i) {
                const int u = i * 256 + t, k = u >> 4, ch = u & 15;
                if (r < 24) {
                    const u16* W = r < 8 ? Wq : (r < 16 ? Wk : Wv);
                    d[i] = *(const v4u*)(W + (size_t)(64 * (r & 7) + k) * HD + h * DH + ch * 8);
                } else {
                    const int kk = (r - 24) >> 2, cb = (r - 24) & 3;
                    const int gg = k >> 4, wi = k & 15, ii = 2 * (wi >> 2) + (wi & 1), cc = (wi >> 1) & 1;
                    const int e = 64 * kk + 32 * cc + 8 * gg + ii;
                    d[i] = *(const v4u*)(Wo + ((size_t)h * DH + e) * DM + cb * 128 + ch * 8);
                }
            }
        };
#pragma unroll
        for (int r = 0; r < 4; ++r) gl(r, P[r]);
#pragma unroll
        for (int r = 0; r < 32; ++r) {
            __syncthreads();
#pragma unroll
            for (int i = 0; i < 4; ++i) { const int u = i * 256 + t; *(LAS v4u*)(L + ST + (u >> 4) * 288 + (u & 15) * 16) = P[r & 3][i]; }
            if (r + 4 < 32) gl(r + 4, P[r & 3]);
            __syncthreads();
            if (r < 24) {
                const int mat = r >> 3, ks = r & 7;
#pragma unroll
                for (int tt = 0; tt < 2; ++tt) {
                    const LAS unsigned char* p = L + ST + (8 * g + (c16 >> 2)) * 288 + (32 * w + 16 * tt + (c16 & 3) * 4) * 2;
                    const bf16x8 lo = cat8(tr_read(p), tr_read(p + 4 * 288));
                    const bf16x8 hi = cat8(tr_read(p + 32 * 288), tr_read(p + 36 * 288));
                    if (mat == 0) Bq[tt][ks] = cat16(lo, hi);
                    else if (mat == 1) Bk[tt][ks] = cat16(lo, hi);
                    else {
                        *(LAS bf16x8*)(L + C_WV + (((2 * w + tt) * 8 + ks) * 2 + 0) * 1024 + l * 16) = lo;
                        *(LAS bf16x8*)(L + C_WV + (((2 * w + tt) * 8 + ks) * 2 + 1) * 1024 + l * 16) = hi;
                    }
                }
            } else {
                const int kk = (r - 24) >> 2, cb = (r - 24) & 3;
                if (w == cb) {
#pragma unroll
                    for (int tt = 0; tt < 8; ++tt) {
                        const LAS unsigned char* p = L + ST + (8 * g + (c16 >> 2)) * 288 + (16 * tt + (c16 & 3) * 4) * 2;
                        const bf16x8 lo = cat8(tr_read(p), tr_read(p + 4 * 288));
                        const bf16x8 hi = cat8(tr_read(p + 32 * 288), tr_read(p + 36 * 288));
                        Bo[tt][kk] = cat16(lo, hi);
                    }
                }
            }
        }
        __syncthreads();
    }
    {
#pragma unroll
        for (int i = 0; i < 2; ++i) {
            const int u = i * 256 + t, ks = u >> 6, ll = u & 63, gg = ll >> 4, cc = (ll >> 3) & 1, m = ll & 7;
            const u16* src = mem0 + m * DM + 64 * ks + 16 * gg + 2 * cc;
            v4u v;
#pragma unroll
            for (int q = 0; q < 4; ++q) v[q] = *(const unsigned*)(src + 4 * q);
            *(LAS v4u*)(L + C_MEM + u * 16) = v;
        }
        wait_flag_kv(flags + FL_QKV + h * 16);
#pragma unroll
        for (int i = 0; i < 2; ++i) {
            const int u = i * 256 + t, r = u >> 4, ch = u & 15;
            *(LAS v4u*)(L + C_KX + r * RST + ch * 16) = ld16_sc1(rK, (unsigned)(r * HD + h * DH + ch * 8) * 2u);
            *(LAS v4u*)(L + C_VX + r * RST + ch * 16) = ld16_sc1(rV, (unsigned)(r * HD + h * DH + ch * 8) * 2u);
        }
    }
    __syncthreads();

#define PIN(x) asm volatile("" : "+v"(x))
    for (int s = 0; s < NSEG; ++s) {
        int t = threadIdx.x; PIN(t);
        const int l = t & 63, g = l >> 4, c16 = l & 15;
        unsigned oWV = (unsigned)(size_t)(L + C_WV + (2 * w * 16) * 1024 + l * 16);
        unsigned oMEM = (unsigned)(size_t)(L + C_MEM + l * 16);
        unsigned oKX = (unsigned)(size_t)(L + C_KX + c16 * RST + 16 * g);
        unsigned oQK = (unsigned)(size_t)(L + C_QS + (c16 & 7) * RST + 16 * g);
        unsigned oQW = (unsigned)(size_t)(L + C_QS + 4 * (g & 1) * RST + (32 * w + 16 * (l >> 5) + c16) * 2);
        unsigned oVW = (unsigned)(size_t)(L + C_VS + (32 * w + 16 * (l >> 5) + c16) * 16 + (g & 1) * 8);
        PIN(oWV); PIN(oMEM); PIN(oKX); PIN(oQK); PIN(oQW); PIN(oVW);
#define LP(o) ((LAS unsigned char*)(size_t)(o))
        f32x4 aq[2], ak[2], av[2];
#pragma unroll
        for (int i = 0; i < 2; ++i) { aq[i] = (f32x4){0.f, 0.f, 0.f, 0.f}; ak[i] = aq[i]; av[i] = aq[i]; PIN(aq[i]); PIN(ak[i]); PIN(av[i]); }
        bf16x8 A[8], Vb[2][4];
#pragma unroll
        for (int i = 0; i < 4; ++i) Vb[0][i] = *(const LAS bf16x8*)(LP(oWV) + ((i >> 1) * 16 + (i & 1)) * 1024);
        if (s > 0) {
            const unsigned slot = (s - 1) & 1;
            dly(DLY2);
            v4u x[2];
            unsigned spins = 0;
            for (;;) {
                asm volatile("" ::: "memory");
                bool ok = true;
#pragma unroll
                for (int i = 0; i < 2; ++i) x[i] = ld16_sc1(rAG, slot * 8192u + (i * 256 + t) * 16u);
#pragma unroll
                for (int i = 0; i < 2; ++i) ok &= (x[i][0] != SENT) & (x[i][1] != SENT) & (x[i][2] != SENT) & (x[i][3] != SENT);
                if (__all(ok)) break;
                if (++spins > SPIN_CAP) break;
            }
#pragma unroll
            for (int i = 0; i < 2; ++i) *(LAS v4u*)(LP(oMEM) + (i * 256 + w * 64) * 16) = x[i];
            STAMP(6);
            __syncthreads();
            STAMP(7);
            if (t == 0) __hip_atomic_fetch_add(flags + FL_PROG, 1u, __ATOMIC_RELAXED, __HIP_MEMORY_SCOPE_AGENT);
        }
#pragma unroll
        for (int ks = 0; ks < 2; ++ks) A[ks] = *(const LAS bf16x8*)(LP(oMEM) + ks * 1024);
        SBAR();
#pragma unroll
        for (int ks = 0; ks < 8; ++ks) {
            if (ks < 6) A[ks + 2] = *(const LAS bf16x8*)(LP(oMEM) + (ks + 2) * 1024);
            if (ks + 1 < 8) {
#pragma unroll
                for (int i = 0; i < 4; ++i) Vb[(ks + 1) & 1][i] = *(const LAS bf16x8*)(LP(oWV) + ((i >> 1) * 16 + (ks + 1) * 2 + (i & 1)) * 1024);
            }
            SBAR();
            aq[0] = SMFMAC(A[ks], Bq[0][ks], aq[0], sidx);
            aq[1] = SMFMAC(A[ks], Bq[1][ks], aq[1], sidx);
            ak[0] = SMFMAC(A[ks], Bk[0][ks], ak[0], sidx);
            ak[1] = SMFMAC(A[ks], Bk[1][ks], ak[1], sidx);
            av[0] = SMFMAC(A[ks], cat16(Vb[ks & 1][0], Vb[ks & 1][1]), av[0], sidx);
            av[1] = SMFMAC(A[ks], cat16(Vb[ks & 1][2], Vb[ks & 1][3]), av[1], sidx);
            SBAR();
        }
        bf16x8 kx[2][4];
#pragma unroll
        for (int kq = 0; kq < 4; ++kq) {
            kx[0][kq] = *(const LAS bf16x8*)(LP(oKX) + 64 * kq);
            kx[1][kq] = *(const LAS bf16x8*)(LP(oKX) + 16 * RST + 64 * kq);
        }
        {
            const f32x4 q = comb2(aq[0], aq[1]), k = comb2(ak[0], ak[1]), v = comb2(av[0], av[1]);
            const unsigned q01 = pk2(q[0], q[1]), q23 = pk2(q[2], q[3]), k01 = pk2(k[0], k[1]), k23 = pk2(k[2], k[3]);
            LAS unsigned char* pq = LP(oQW);
            LAS unsigned char* pk = LP(oQW) + (C_KS - C_QS);
            *(LAS u16*)(pq) = (u16)q01; *(LAS u16*)(pq + RST) = (u16)(q01 >> 16); *(LAS u16*)(pq + 2 * RST) = (u16)q23; *(LAS u16*)(pq + 3 * RST) = (u16)(q23 >> 16);
            *(LAS u16*)(pk) = (u16)k01; *(LAS u16*)(pk + RST) = (u16)(k01 >> 16); *(LAS u16*)(pk + 2 * RST) = (u16)k23; *(LAS u16*)(pk + 3 * RST) = (u16)(k23 >> 16);
            v2u vv = {pk2(v[0], v[1]), pk2(v[2], v[3])};
            *(LAS v2u*)(LP(oVW)) = vv;
        }
        unsigned oVX = (unsigned)(size_t)(L + C_VX + (4 * g + (c16 >> 2)) * RST + (c16 & 3) * 8 + 64 * w);
        unsigned oVM = (unsigned)(size_t)(L + C_VS + c16 * 16 + (g & 1) * 8 + 512 * w);
        unsigned oOW = (unsigned)(size_t)(L + C_MEM + (c16 & 7) * RST + (32 * w + 4 * g) * 2);
        unsigned oOR = (unsigned)(size_t)(L + C_MEM + (c16 & 7) * RST + (32 * (c16 >> 3) + 8 * g) * 2);
        PIN(oVX); PIN(oVM); PIN(oOW); PIN(oOR);
        f32x4 oo[8];
        STAMP(0);
        __syncthreads();
        STAMP(1);
        if (s == NSEG - 1) break;
        bf16x8 aO[2];
        {
            bf16x8 qb[4], km_[4];
#pragma unroll
            for (int kq = 0; kq < 4; ++kq) {
                qb[kq] = *(const LAS bf16x8*)(LP(oQK) + 64 * kq);
                km_[kq] = *(const LAS bf16x8*)(LP(oQK) + (C_KS - C_QS) + 64 * kq);
            }
            bf16x8 va[2]; v2u vm[2];
#pragma unroll
            for (int T = 0; T < 2; ++T) {
                va[T] = cat8(tr_read(LP(oVX) + 32 * T), tr_read(LP(oVX) + 32 * T + 16 * RST));
                vm[T] = *(const LAS v2u*)(LP(oVM) + 256 * T);
            }
            SBAR();
            f32x4 sc[3];
#pragma unroll
            for (int T = 0; T < 3; ++T) sc[T] = (f32x4){0.f, 0.f, 0.f, 0.f};
#pragma unroll
            for (int kq = 0; kq < 4; ++kq) {
                sc[0] = MFMA32(kx[0][kq], qb[kq], sc[0]); sc[1] = MFMA32(kx[1][kq], qb[kq], sc[1]); sc[2] = MFMA32(km_[kq], qb[kq], sc[2]);
            }
            SBAR();
            const float cs = 0.08838834764831845f * 1.4426950408889634f;
            if (g >= 2) sc[2] = (f32x4){-3.0e38f, -3.0e38f, -3.0e38f, -3.0e38f};
            float mx = fmaxf(fmaxf(fmaxf(sc[0][0], sc[0][1]), fmaxf(sc[0][2], sc[0][3])), fmaxf(fmaxf(sc[1][0], sc[1][1]), fmaxf(sc[1][2], sc[1][3])));
            mx = fmaxf(mx, fmaxf(fmaxf(sc[2][0], sc[2][1]), fmaxf(sc[2][2], sc[2][3])));
            mx = xl32_max(xl16_max(mx));
            const float nm = -mx * cs;
            float p[12];
#pragma unroll
            for (int T = 0; T < 3; ++T)
#pragma unroll
                for (int r = 0; r < 4; ++r) p[T * 4 + r] = __builtin_amdgcn_exp2f(__builtin_fmaf(sc[T][r], cs, nm));
            float sum = ((p[0] + p[1]) + (p[2] + p[3])) + ((p[4] + p[5]) + (p[6] + p[7])) + ((p[8] + p[9]) + (p[10] + p[11]));
            sum = xl32_add(xl16_add(sum));
            const float inv = __builtin_amdgcn_rcpf(sum);
            v4u px = {pk2(p[0] * inv, p[1] * inv), pk2(p[2] * inv, p[3] * inv), pk2(p[4] * inv, p[5] * inv), pk2(p[6] * inv, p[7] * inv)};
            const bf16x8 Px = __builtin_bit_cast(bf16x8, px);
            const bf16x8 Pm = __builtin_bit_cast(bf16x8, (v4u){pk2(p[8] * inv, p[9] * inv), pk2(p[10] * inv, p[11] * inv), 0u, 0u});
            f32x4 o[2];
#pragma unroll
            for (int T = 0; T < 2; ++T) o[T] = MFMA32(va[T], Px, ((f32x4){0.f, 0.f, 0.f, 0.f}));
#pragma unroll
            for (int T = 0; T < 2; ++T) o[T] = MFMA32(__builtin_bit_cast(bf16x8, (v4u){vm[T][0], vm[T][1], 0u, 0u}), Pm, o[T]);
            if (c16 < 8) {
#pragma unroll
                for (int T = 0; T < 2; ++T) *(LAS v2u*)(LP(oOW) + 32 * T) = (v2u){pk2(o[T][0], o[T][1]), pk2(o[T][2], o[T][3])};
            }
#pragma unroll
            for (int tt = 0; tt < 8; ++tt) { oo[tt] = (f32x4){0.f, 0.f, 0.f, 0.f}; PIN(oo[tt]); }
            __syncthreads();
            aO[0] = *(const LAS bf16x8*)(LP(oOR));
            aO[1] = *(const LAS bf16x8*)(LP(oOR) + 128);
        }
        STAMP(2);
        const unsigned slot = s & 1, par = (s >> 1) & 1;
        {
#pragma unroll
            for (int tt = 0; tt < 8; ++tt) oo[tt] = SMFMAC(aO[0], Bo[tt][0], oo[tt], sidx);
#pragma unroll
            for (int tt = 0; tt < 8; ++tt) oo[tt] = SMFMAC(aO[1], Bo[tt][1], oo[tt], sidx);
#pragma unroll
            for (int tp = 0; tp < 4; ++tp) {
                const f32x4 o = comb2(oo[2 * tp], oo[2 * tp + 1]);
                v4u st;
#pragma unroll
                for (int r = 0; r < 4; ++r) st[r] = (__float_as_uint(o[r]) & ~1u) | par;
                const unsigned cons = 8 * w + 2 * tp + (l >> 5);
                st16_sc1(st, rRS, slot * 524288u + cons * 16384u + h * 512u + (l & 31) * 16u);
            }
        }
        STAMP(3);
        {
            const int u = t & 127;
            const v4u pub = *(const LAS v4u*)(L + (t < 128 ? C_KS + (u >> 4) * RST + (u & 15) * 16 : C_VS + u * 16));
            const unsigned off = (unsigned)((s * NH + h) * (MM * DH) + u * 8) * 2u;
            if (t < 128) st16_sc1(pub, rKM, off); else st16_sc1(pub, rVM, off);
        }
        if (((s + 1) & 3) == 0) wait_flag_kv(flags + FL_QKV + (((s + 1) >> 2) * 32 + h) * 16);
        v4u pk_[2], pv_[2];
#pragma unroll
        for (int i = 0; i < 2; ++i) {
            const int u = i * 256 + t, r = u >> 4, ch = u & 15;
            pk_[i] = ld16_sc1(rK, (unsigned)(((s + 1) * SEG + r) * HD + h * DH + ch * 8) * 2u);
            pv_[i] = ld16_sc1(rV, (unsigned)(((s + 1) * SEG + r) * HD + h * DH + ch * 8) * 2u);
        }
        const int lr = (l >> 3) & 3;
        unsigned offAG;
        {
            const int m = 4 * (w >> 1) + lr, cp = 4 * (w & 1) + ((l & 7) >> 1);
            offAG = slot * 8192u + (h >> 2) * 1024u + ((h & 3) * 16 + (cp & 1) * 8 + m) * 16u + (cp >> 1) * 4u;
            PIN(offAG);
        }
        dly(DLY1);
        {
            v4u x[4];
            unsigned spins = 0;
            const unsigned base = slot * 524288u + h * 16384u + (l >> 3) * 512u + (8 * w + (l & 7)) * 16u;
            for (;;) {
                asm volatile("" ::: "memory");
                unsigned bad = 0;
#pragma unroll
                for (int i = 0; i < 4; ++i) x[i] = ld16_sc1(rRS, base + i * 4096u);
#pragma unroll
                for (int i = 0; i < 4; ++i) bad |= (x[i][0] ^ par) | (x[i][1] ^ par) | (x[i][2] ^ par) | (x[i][3] ^ par);
                if (__all((bad & 1u) == 0)) break;
                if (++spins > SPIN_CAP) break;
            }
            STAMP(4);
            float sm[4];
#pragma unroll
            for (int r = 0; r < 4; ++r) {
                float v = (__uint_as_float(x[0][r]) + __uint_as_float(x[1][r])) + (__uint_as_float(x[2][r]) + __uint_as_float(x[3][r]));
                sm[r] = xl32_add(xl16_add(xl8_add(v)));
            }
            const float own = lr == 0 ? sm[0] : (lr == 1 ? sm[1] : (lr == 2 ? sm[2] : sm[3]));
            const float nb = xl1_get(own);
            unsigned pkd = pk2(own, nb);
            if (pkd == SENT) pkd = 0x7FC07FC0u;
            if (l < 32 && !(l & 1)) {
                st4_sc1(pkd, rAG, offAG);
                st4_sc1(SENT, rAG, offAG ^ 8192u);
            }
        }
        STAMP(5);
#pragma unroll
        for (int i = 0; i < 2; ++i) {
            const int u = i * 256 + t, r = u >> 4, ch = u & 15;
            *(LAS v4u*)(L + C_KX + r * RST + ch * 16) = pk_[i];
            *(LAS v4u*)(L + C_VX + r * RST + ch * 16) = pv_[i];
        }
    }
    {
        const int u = t & 127;
        const v4u pub = *(const LAS v4u*)(L + (t < 128 ? C_KS + (u >> 4) * RST + (u & 15) * 16 : C_VS + u * 16));
        const unsigned off = (unsigned)(((NSEG - 1) * NH + h) * (MM * DH) + u * 8) * 2u;
        if (t < 128) st16_sc1(pub, rKM, off); else st16_sc1(pub, rVM, off);
        asm volatile("s_waitcnt vmcnt(0)" ::: "memory");
        __syncthreads();
        if (t == 0) __hip_atomic_fetch_add(flags + FL_PROG, 1u, __ATOMIC_RELAXED, __HIP_MEMORY_SCOPE_AGENT);
    }
}

__device__ __forceinline__ void xattn_task(unsigned char* smem, const u16* Q, const u16* Kx, const u16* Vx,
                                           const u16* kmem, const u16* vmem, u16* __restrict__ O, int s, int h) {
    const int t = threadIdx.x, w = t >> 6, l = t & 63, g = l >> 4, c16 = l & 15;
    LAS unsigned char* LV = (LAS unsigned char*)smem + w * 32 * 288;
    const size_t rowb = (size_t)s * SEG;
#pragma unroll
    for (int i = 0; i < 8; ++i) {
        const int u = i * 64 + l, r = u >> 4, ch = u & 15;
        *(LAS v4u*)(LV + r * 288 + ch * 16) = *(const v4u*)(Vx + (rowb + r) * HD + h * DH + ch * 8);
    }
    f32x4 sc[2][3];
#pragma unroll
    for (int qt = 0; qt < 2; ++qt)
#pragma unroll
        for (int T = 0; T < 3; ++T) sc[qt][T] = (f32x4){0.f, 0.f, 0.f, 0.f};
#pragma unroll
    for (int kq = 0; kq < 4; ++kq) {
        const int eo = 32 * kq + 8 * g;
        const bf16x8 q0 = *(const bf16x8*)(Q + (rowb + c16) * HD + h * DH + eo);
        const bf16x8 q1 = *(const bf16x8*)(Q + (rowb + 16 + c16) * HD + h * DH + eo);
        const bf16x8 k0 = *(const bf16x8*)(Kx + (rowb + c16) * HD + h * DH + eo);
        const bf16x8 k1 = *(const bf16x8*)(Kx + (rowb + 16 + c16) * HD + h * DH + eo);
        const bf16x8 k2 = *(const bf16x8*)(kmem + ((size_t)(s * NH + h) * MM + (c16 & 7)) * DH + eo);
        sc[0][0] = MFMA32(k0, q0, sc[0][0]); sc[0][1] = MFMA32(k1, q0, sc[0][1]); sc[0][2] = MFMA32(k2, q0, sc[0][2]);
        sc[1][0] = MFMA32(k0, q1, sc[1][0]); sc[1][1] = MFMA32(k1, q1, sc[1][1]); sc[1][2] = MFMA32(k2, q1, sc[1][2]);
    }
    bf16x8 Px[2], Pm[2];
    const float scale = 0.08838834764831845f;
#pragma unroll
    for (int qt = 0; qt < 2; ++qt) {
        const int qi = 16 * qt + c16;
        float p[12]; float mx = -3.0e38f;
#pragma unroll
        for (int T = 0; T < 3; ++T)
#pragma unroll
            for (int r = 0; r < 4; ++r) {
                float v = rbf(rbf(sc[qt][T][r]) * scale);
                const bool valid = (T == 2) ? (g < 2) : (16 * T + 4 * g + r <= qi);
                if (!valid) v = -3.0e38f;
                p[T * 4 + r] = v; mx = fmaxf(mx, v);
            }
        mx = fmaxf(mx, __shfl_xor(mx, 16)); mx = fmaxf(mx, __shfl_xor(mx, 32));
        float sum = 0.f;
#pragma unroll
        for (int i = 0; i < 12; ++i) { p[i] = (p[i] < -1.0e38f) ? 0.f : __expf(p[i] - mx); sum += p[i]; }
        sum += __shfl_xor(sum, 16); sum += __shfl_xor(sum, 32);
        const float inv = 1.0f / sum;
        v4u px = {pk2(p[0] * inv, p[1] * inv), pk2(p[2] * inv, p[3] * inv), pk2(p[4] * inv, p[5] * inv), pk2(p[6] * inv, p[7] * inv)};
        v2u pm = {pk2(p[8] * inv, p[9] * inv), pk2(p[10] * inv, p[11] * inv)};
        Px[qt] = __builtin_bit_cast(bf16x8, px); Pm[qt] = __builtin_bit_cast(bf16x8, (v4u){pm[0], pm[1], 0u, 0u});
    }
    __syncthreads();
#pragma unroll
    for (int T = 0; T < 8; ++T) {
        const LAS unsigned char* pv = LV + (4 * g + (c16 >> 2)) * 288 + (16 * T + (c16 & 3) * 4) * 2;
        const bf16x8 va = cat8(tr_read(pv), tr_read(pv + 16 * 288));
        v2u vm = *(const v2u*)(vmem + (size_t)(s * NH + h) * (MM * DH) + (16 * T + c16) * 8 + (g & 1) * 4);
        if (g >= 2) vm = (v2u){0u, 0u};
#pragma unroll
        for (int qt = 0; qt < 2; ++qt) {
            f32x4 o = (f32x4){0.f, 0.f, 0.f, 0.f};
            o = MFMA32(va, Px[qt], o);
            o = MFMA32(__builtin_bit_cast(bf16x8, (v4u){vm[0], vm[1], 0u, 0u}), Pm[qt], o);
            v2u st = {pk2(o[0], o[1]), pk2(o[2], o[3])};
            *(v2u*)(O + (rowb + 16 * qt + c16) * HD + h * DH + 16 * T + 4 * g) = st;
        }
    }
}

constexpr int NBULK = 224;
__global__ void __launch_bounds__(256, 1) k_fused(const u16* __restrict__ Wq, const u16* __restrict__ Wk, const u16* __restrict__ Wv,
                                                  const u16* __restrict__ Wo, const u16* __restrict__ mem0, const u16* __restrict__ xn,
                                                  u16* Q, u16* Kx, u16* Vx, u16* kmem, u16* vmem, u16* O, unsigned char* ws_sync) {
    extern __shared__ __attribute__((aligned(16))) unsigned char smem[];
    if (blockIdx.x < NH) {
        chain_body(smem, Wq, Wk, Wv, Wo, mem0, Kx, Vx, kmem, vmem, ws_sync);
        return;
    }
    unsigned* flags = (unsigned*)(ws_sync + WS_FLAGS);
    const int b = blockIdx.x - NH, t = threadIdx.x, w = __builtin_amdgcn_readfirstlane(t >> 6);
    for (int i = b; i < 16 * 96; i += NBULK) {
        int rb, hh, mat;
        if (i < 1024) { rb = i >> 6; hh = (i >> 1) & 31; mat = 1 + (i & 1); }
        else { const int j = i - 1024; rb = j >> 5; hh = j & 31; mat = 0; }
        const u16* W = mat == 0 ? Wq : (mat == 1 ? Wk : Wv);
        u16* C = mat == 0 ? Q : (mat == 1 ? Kx : Vx);
        gemm_tile<0, true, 128, 128>(smem, xn, DM, W, nullptr, HD, C, HD, nullptr, DM, rb * 128, hh * DH);
        asm volatile("s_waitcnt vmcnt(0)" ::: "memory");
        __syncthreads();
        if (t == 0) __hip_atomic_fetch_add(flags + FL_QKV + (rb * 32 + hh) * 16, mat == 0 ? 4u : 1u, __ATOMIC_RELAXED, __HIP_MEMORY_SCOPE_AGENT);
    }
    for (int j = b; j < NSEG * 8; j += NBULK) {
        const int s = j >> 3, hh = (j & 7) * 4 + w;
        if (w == 0) {
            unsigned spins = 0;
            while ((unsigned)__builtin_amdgcn_readfirstlane(ld_flag(flags + FL_PROG)) < (unsigned)(s + 1) * NH) { __builtin_amdgcn_s_sleep(64); if (++spins > SPIN_CAP) break; }
        }
        wait_flag_ge(flags + FL_QKV + ((s >> 2) * 32 + hh) * 16, 6, 32);
        __syncthreads();
        __builtin_amdgcn_fence(__ATOMIC_ACQUIRE, "agent");
        asm volatile("s_waitcnt vmcnt(0)" ::: "memory");
        xattn_task(smem, Q, Kx, Vx, kmem, vmem, O, s, hh);
        __syncthreads();
    }
}

extern "C" void kernel_launch(void* const* d_in, const int* in_sizes, int n_in, void* d_out, int out_size, void* d_ws, size_t ws_size,
                              hipStream_t stream) {
    const u16* x = (const u16*)d_in[0];
    const u16* mem0 = (const u16*)d_in[1];
    const u16* Wq = (const u16*)d_in[2];
    const u16* Wk = (const u16*)d_in[3];
    const u16* Wv = (const u16*)d_in[4];
    const u16* Wo = (const u16*)d_in[5];
    const u16* wg = (const u16*)d_in[8];
    const u16* wu = (const u16*)d_in[9];
    const u16* wd = (const u16*)d_in[10];
    u16* out = (u16*)d_out;
    unsigned char* ws = (unsigned char*)d_ws;
    u16* xn = (u16*)(ws + WS_XN); u16* Q = (u16*)(ws + WS_Q); u16* K = (u16*)(ws + WS_K); u16* V = (u16*)(ws + WS_V);
    u16* O = (u16*)(ws + WS_O); u16* km = (u16*)(ws + WS_KM); u16* vm = (u16*)(ws + WS_VM);
    u16* H = (u16*)(ws + WS_H); u16* HN = (u16*)(ws + WS_HN); u16* ACT = (u16*)(ws + WS_ACT);

    (void)hipFuncSetAttribute((const void*)k_fused, hipFuncAttributeMaxDynamicSharedMemorySize, C_LDS);
    (void)hipFuncSetAttribute((const void*)k_gemm<2, 128, 128>, hipFuncAttributeMaxDynamicSharedMemorySize, 2 * g_stage(128, 128));
    k_ln<true><<<S_LEN / 4, 256, 0, stream>>>(x, xn, ws);
    k_fused<<<NH + NBULK, 256, C_LDS, stream>>>(Wq, Wk, Wv, Wo, mem0, xn, Q, K, V, km, vm, O, ws);
    k_gemm<1, 64, 64><<<dim3(DM / 64, S_LEN / 64), 256, 2 * g_stage(64, 64), stream>>>(O, HD, Wo, nullptr, DM, H, DM, x, HD);
    k_ln<false><<<S_LEN / 4, 256, 0, stream>>>(H, HN, ws);
    k_gemm<2, 128, 128><<<dim3(FF / 64, S_LEN / 128), 256, 2 * g_stage(128, 128), stream>>>(HN, DM, wg, wu, FF, ACT, FF, nullptr, DM);
    k_gemm<1, 64, 64><<<dim3(DM / 64, S_LEN / 64), 256, 2 * g_stage(64, 64), stream>>>(ACT, FF, wd, nullptr, DM, out, DM, H, FF);
}
```

```cpp
#include <hip/hip_runtime.h>
#include <hip/hip_bf16.h>
#include <stdint.h>

typedef unsigned short u16;
typedef __bf16 bf16x8 __attribute__((ext_vector_type(8)));
typedef __bf16 bf16x4 __attribute__((ext_vector_type(4)));
typedef __bf16 bf16x16 __attribute__((ext_vector_type(16)));
typedef short s16x4 __attribute__((ext_vector_type(4)));
typedef float f32x4 __attribute__((ext_vector_type(4)));
typedef unsigned v4u __attribute__((ext_vector_type(4)));
typedef unsigned v2u __attribute__((ext_vector_type(2)));
#define LAS __attribute__((address_space(3)))

constexpr int S_LEN = 2048, DM = 512, NH = 32, DH = 128, HD = NH * DH, MM = 8, SEG = 32, NSEG = S_LEN / SEG, FF = 1792;

constexpr size_t WS_RS = 0;
constexpr size_t WS_AG = WS_RS + 2u * 32 * 32 * 512;
constexpr size_t WS_SYNC_END = WS_AG + 2u * 8192;
constexpr size_t WS_FLAGS = WS_SYNC_END;
constexpr int FL_QKV = 0;
constexpr int FL_PROG = 16 * 32 * 16;
constexpr int FL_GRP = FL_PROG + 64;
constexpr int FL_WORDS = FL_GRP + 16 * 4 * 16;
constexpr size_t WS_FLAGS_END = WS_FLAGS + FL_WORDS * 4;
constexpr size_t WS_XN = 2u << 20;
constexpr size_t WS_Q = WS_XN + (size_t)S_LEN * DM * 2;
constexpr size_t WS_K = WS_Q + (size_t)S_LEN * HD * 2;
constexpr size_t WS_V = WS_K + (size_t)S_LEN * HD * 2;
constexpr size_t WS_O = WS_V + (size_t)S_LEN * HD * 2;
constexpr size_t WS_KM = WS_O + (size_t)S_LEN * HD * 2;
constexpr size_t WS_VM = WS_KM + (size_t)NSEG * NH * MM * DH * 2;
constexpr size_t WS_H = WS_VM + (size_t)NSEG * NH * MM * DH * 2;
constexpr size_t WS_HN = WS_H + (size_t)S_LEN * DM * 2;
constexpr size_t WS_ACT = WS_HN + (size_t)S_LEN * DM * 2;
constexpr size_t WS_SLAB = WS_ACT + (size_t)S_LEN * FF * 2;
constexpr size_t WS_END = WS_SLAB + (size_t)8 * S_LEN * DM * 4;

__device__ __forceinline__ float bf2f(u16 v) { return __uint_as_float((unsigned)v << 16); }
__device__ __forceinline__ u16 f2bf(float f) { return __builtin_bit_cast(u16, (__bf16)f); }
__device__ __forceinline__ float rbf(float f) { return (float)(__bf16)f; }
typedef __bf16 bf16x2 __attribute__((ext_vector_type(2)));
typedef float f32x2 __attribute__((ext_vector_type(2)));
__device__ __forceinline__ unsigned pk2(float lo, float hi) {
    const f32x2 v = {lo, hi};
    return __builtin_bit_cast(unsigned, __builtin_convertvector(v, bf16x2));
}
__device__ __forceinline__ float xl32_add(float v) { auto r = __builtin_amdgcn_permlane32_swap(__float_as_uint(v), __float_as_uint(v), false, false); return __uint_as_float(r[0]) + __uint_as_float(r[1]); }
__device__ __forceinline__ float xl16_add(float v) { auto r = __builtin_amdgcn_permlane16_swap(__float_as_uint(v), __float_as_uint(v), false, false); return __uint_as_float(r[0]) + __uint_as_float(r[1]); }
__device__ __forceinline__ float xl32_max(float v) { auto r = __builtin_amdgcn_permlane32_swap(__float_as_uint(v), __float_as_uint(v), false, false); return fmaxf(__uint_as_float(r[0]), __uint_as_float(r[1])); }
__device__ __forceinline__ float xl16_max(float v) { auto r = __builtin_amdgcn_permlane16_swap(__float_as_uint(v), __float_as_uint(v), false, false); return fmaxf(__uint_as_float(r[0]), __uint_as_float(r[1])); }
__device__ __forceinline__ float xl8_add(float v) { return v + __uint_as_float(__builtin_amdgcn_update_dpp(0, __float_as_uint(v), 0x128, 0xf, 0xf, false)); }
__device__ __forceinline__ float xl1_get(float v) { return __uint_as_float(__builtin_amdgcn_update_dpp(0, __float_as_uint(v), 0xB1, 0xf, 0xf, false)); }
#define SBAR() __builtin_amdgcn_sched_barrier(0)
__device__ __forceinline__ s16x4 tr_read(const LAS unsigned char* p) {
    return __builtin_amdgcn_ds_read_tr16_b64_v4i16((LAS s16x4*)p);
}
__device__ __forceinline__ bf16x8 cat8(s16x4 a, s16x4 b) {
    typedef short s16x8 __attribute__((ext_vector_type(8)));
    s16x8 r = __builtin_shufflevector(a, b, 0, 1, 2, 3, 4, 5, 6, 7);
    return __builtin_bit_cast(bf16x8, r);
}
__device__ __forceinline__ bf16x16 cat16(bf16x8 a, bf16x8 b) {
    return __builtin_shufflevector(a, b, 0, 1, 2, 3, 4, 5, 6, 7, 8, 9, 10, 11, 12, 13, 14, 15);
}
#define MFMA32(a, b, c) __builtin_amdgcn_mfma_f32_16x16x32_bf16((a), (b), (c), 0, 0, 0)
#define SMFMAC(a, b, c, idx) __builtin_amdgcn_smfmac_f32_16x16x64_bf16((a), (b), (c), (idx), 0, 0)

template <bool INIT>
__global__ void __launch_bounds__(256) k_ln(const u16* __restrict__ x, u16* __restrict__ y, unsigned char* __restrict__ ws) {
    const int row = blockIdx.x * 4 + (threadIdx.x >> 6), lane = threadIdx.x & 63;
    if (INIT) {
        const size_t u = (size_t)blockIdx.x * 256 + threadIdx.x;
        if (u * 16 < WS_SYNC_END) *(v4u*)(ws + u * 16) = (v4u){0xFFFFFFFFu, 0xFFFFFFFFu, 0xFFFFFFFFu, 0xFFFFFFFFu};
        else if (u * 16 < WS_FLAGS_END) *(v4u*)(ws + u * 16) = (v4u){0u, 0u, 0u, 0u};
    }
    const v4u raw = *(const v4u*)(x + (size_t)row * DM + lane * 8);
    float v[8];
#pragma unroll
    for (int i = 0; i < 4; ++i) { v[2 * i] = __uint_as_float(raw[i] << 16); v[2 * i + 1] = __uint_as_float(raw[i] & 0xffff0000u); }
    float s = 0.f;
#pragma unroll
    for (int i = 0; i < 8; ++i) s += v[i];
#pragma unroll
    for (int o = 32; o >= 1; o >>= 1) s += __shfl_xor(s, o);
    const float mean = s * (1.0f / DM);
    float q = 0.f;
#pragma unroll
    for (int i = 0; i < 8; ++i) { const float d = v[i] - mean; q += d * d; }
#pragma unroll
    for (int o = 32; o >= 1; o >>= 1) q += __shfl_xor(q, o);
    const float mb = rbf(mean), vb = rbf(q * (1.0f / DM));
    const float den = rbf(sqrtf(rbf(vb + 1e-5f)));
    v4u o;
#pragma unroll
    for (int i = 0; i < 4; ++i) o[i] = pk2(rbf(v[2 * i] - mb) / den, rbf(v[2 * i + 1] - mb) / den);
    *(v4u*)(y + (size_t)row * DM + lane * 8) = o;
}

constexpr int G_BK = 64, G_AST = 144;
__host__ __device__ constexpr int g_stage(int BM, int BN) { return BM * G_AST + G_BK * (BN * 2 + 32); }
__device__ __forceinline__ void st8_dev(u16* p, v2u v) {
    __hip_atomic_store((unsigned long long*)p, (unsigned long long)v[0] | ((unsigned long long)v[1] << 32), __ATOMIC_RELAXED, __HIP_MEMORY_SCOPE_AGENT);
}
template <int EPI, bool DEV, int BM, int BN>
__device__ __forceinline__ void gemm_tile(unsigned char* smem, const u16* __restrict__ A, int lda, const u16* __restrict__ B, const u16* __restrict__ B2, int ldb,
                                          u16* __restrict__ C, int ldc, const u16* __restrict__ res, int K, int m0, int n0) {
    constexpr int BST = BN * 2 + 32, ABYTES = BM * G_AST, STAGE = g_stage(BM, BN);
    constexpr int NA = BM / 32, NB = BN / 32, MI = BM / 32, NJ = BN / 32, BCH = BN / 8;
    const int t = threadIdx.x, w = t >> 6, l = t & 63, g = l >> 4, c16 = l & 15;
    const int wr = w >> 1, wc = w & 1;
    v4u ra[4][NA], rb[4][NB];
    auto gload = [&](int k0, v4u (&da)[NA], v4u (&db)[NB]) {
#pragma unroll
        for (int i = 0; i < NA; ++i) { const int u = i * 256 + t; da[i] = *(const v4u*)(A + (size_t)(m0 + (u >> 3)) * lda + k0 + (u & 7) * 8); }
#pragma unroll
        for (int i = 0; i < NB; ++i) {
            const int u = i * 256 + t, kr = u / BCH, ch = u % BCH;
            if (EPI == 2) {
                constexpr int Q = BCH / 4;
                const int wcc = ch / (2 * Q), part = (ch / Q) & 1, cc = (ch % Q) * 8;
                db[i] = *(const v4u*)((part ? B2 : B) + (size_t)(k0 + kr) * ldb + n0 + (BN / 4) * wcc + cc);
            } else db[i] = *(const v4u*)(B + (size_t)(k0 + kr) * ldb + n0 + ch * 8);
        }
    };
    auto swrite = [&](int buf, v4u (&da)[NA], v4u (&db)[NB]) {
        unsigned char* sa = smem + buf * STAGE; unsigned char* sb = sa + ABYTES;
#pragma unroll
        for (int i = 0; i < NA; ++i) { const int u = i * 256 + t; *(v4u*)(sa + (u >> 3) * G_AST + (u & 7) * 16) = da[i]; }
#pragma unroll
        for (int i = 0; i < NB; ++i) { const int u = i * 256 + t; *(v4u*)(sb + (u / BCH) * BST + (u % BCH) * 16) = db[i]; }
    };
    f32x4 acc[MI][NJ];
#pragma unroll
    for (int i = 0; i < MI; ++i)
#pragma unroll
        for (int j = 0; j < NJ; ++j) acc[i][j] = (f32x4){0.f, 0.f, 0.f, 0.f};
    bf16x8 af[2][MI], bfr[2][NJ];
    auto fread = [&](int buf) {
        const LAS unsigned char* sa = (const LAS unsigned char*)smem + buf * STAGE;
        const LAS unsigned char* sb = sa + ABYTES;
#pragma unroll
        for (int ks = 0; ks < 2; ++ks) {
#pragma unroll
            for (int i = 0; i < MI; ++i) af[ks][i] = *(const LAS bf16x8*)(sa + (wr * (BM / 2) + i * 16 + c16) * G_AST + (ks * 32 + g * 8) * 2);
#pragma unroll
            for (int j = 0; j < NJ; ++j) {
                const LAS unsigned char* p = sb + (ks * 32 + g * 8 + (c16 >> 2)) * BST + (wc * (BN / 2) + j * 16 + (c16 & 3) * 4) * 2;
                bfr[ks][j] = cat8(tr_read(p), tr_read(p + 4 * BST));
            }
        }
    };
    auto mma = [&](int ks) {
#pragma unroll
        for (int i = 0; i < MI; ++i)
#pragma unroll
            for (int j = 0; j < NJ; ++j) acc[i][j] = MFMA32(bfr[ks][j], af[ks][i], acc[i][j]);
    };
    const int nk = K / G_BK;
    const int klast = K - G_BK;
#pragma unroll
    for (int i = 0; i < 4; ++i) gload(i * G_BK, ra[i], rb[i]);
    __syncthreads();
    swrite(0, ra[0], rb[0]);
    gload(min(4 * G_BK, klast), ra[0], rb[0]);
    __syncthreads();
    for (int kt = 0; kt < nk; kt += 4) {
#pragma unroll
        for (int u = 0; u < 4; ++u) {
            fread(u & 1);
            mma(0);
            swrite((u + 1) & 1, ra[(u + 1) & 3], rb[(u + 1) & 3]);
            gload(min((kt + u + 5) * G_BK, klast), ra[(u + 1) & 3], rb[(u + 1) & 3]);
            mma(1);
            __syncthreads();
        }
    }
    if (EPI == 4) {
        const auto rC = __builtin_amdgcn_make_buffer_rsrc((void*)C, 0, (unsigned)S_LEN * DM * 4, 0x00020000);
#pragma unroll
        for (int i = 0; i < MI; ++i)
#pragma unroll
            for (int j = 0; j < NJ; ++j)
                __builtin_amdgcn_raw_buffer_store_b128(__builtin_bit_cast(v4u, acc[i][j]), rC, (unsigned)((m0 + wr * (BM / 2) + i * 16 + c16) * ldc + n0 + wc * (BN / 2) + j * 16 + 4 * g) * 4u, 0, DEV ? 16 : 0);
        return;
    }
#pragma unroll
    for (int i = 0; i < MI; ++i) {
        const int m = m0 + wr * (BM / 2) + i * 16 + c16;
        if (EPI == 2) {
#pragma unroll
            for (int j = 0; j < NJ / 2; ++j) {
                float o[4];
#pragma unroll
                for (int r = 0; r < 4; ++r) {
                    const float gt = rbf(acc[i][j][r]), up = rbf(acc[i][j + NJ / 2][r]);
                    const float inner = 0.7978845608028654f * (gt + 0.044715f * gt * gt * gt);
                    const float th = 1.0f - 2.0f * __builtin_amdgcn_rcpf(__expf(2.0f * inner) + 1.0f);
                    const float ge = rbf(0.5f * gt * (1.0f + th));
                    o[r] = ge * up;
                }
                v2u st = {pk2(o[0], o[1]), pk2(o[2], o[3])};
                u16* dst = C + (size_t)m * ldc + n0 + wc * (BN / 4) + j * 16 + 4 * g;
                if (DEV) st8_dev(dst, st); else *(v2u*)dst = st;
            }
        } else {
#pragma unroll
            for (int j = 0; j < NJ; ++j) {
                const int n = n0 + wc * (BN / 2) + j * 16 + 4 * g;
                float o[4];
#pragma unroll
                for (int r = 0; r < 4; ++r) o[r] = acc[i][j][r];
                if (EPI == 1) {
                    const v2u rr = *(const v2u*)(res + (size_t)m * ldc + n);
                    o[0] = __uint_as_float(rr[0] << 16) + rbf(o[0]); o[1] = __uint_as_float(rr[0] & 0xffff0000u) + rbf(o[1]);
                    o[2] = __uint_as_float(rr[1] << 16) + rbf(o[2]); o[3] = __uint_as_float(rr[1] & 0xffff0000u) + rbf(o[3]);
                }
                v2u st = {pk2(o[0], o[1]), pk2(o[2], o[3])};
                if (DEV) st8_dev(C + (size_t)m * ldc + n, st); else *(v2u*)(C + (size_t)m * ldc + n) = st;
            }
        }
    }
}
template <int EPI, int BM, int BN>
__global__ void __launch_bounds__(256) k_gemm(const u16* __restrict__ A, int lda, const u16* __restrict__ B, const u16* __restrict__ B2, int ldb,
                                              u16* __restrict__ C, int ldc, const u16* __restrict__ res, int K, int row0) {
    extern __shared__ __attribute__((aligned(16))) unsigned char smem[];
    gemm_tile<EPI, false, BM, BN>(smem, A, lda, B, B2, ldb, C, ldc, res, K, row0 + blockIdx.y * BM, blockIdx.x * (EPI == 2 ? BN / 2 : BN));
}

template <int BM, int BN>
__global__ void __launch_bounds__(256) k_gemm_splitk(const u16* __restrict__ A, int lda, const u16* __restrict__ B, int ldb, float* __restrict__ slab, int Kpart, int row0) {
    extern __shared__ __attribute__((aligned(16))) unsigned char smem[];
    const int z = blockIdx.z;
    gemm_tile<4, false, BM, BN>(smem, A + (size_t)z * Kpart, lda, B + (size_t)z * Kpart * ldb, nullptr, ldb, (u16*)(slab + (size_t)z * S_LEN * DM), DM, nullptr, Kpart, row0 + blockIdx.y * BM, blockIdx.x * BN);
}
template <int NS, bool LN>
__global__ void __launch_bounds__(256) k_reduce(const float* __restrict__ slab, const u16* __restrict__ res, u16* __restrict__ hout, u16* __restrict__ nout, int row0) {
    const int row = row0 + blockIdx.x * 4 + (threadIdx.x >> 6), l = threadIdx.x & 63;
    f32x4 a0 = (f32x4){0.f, 0.f, 0.f, 0.f}, a1 = a0;
#pragma unroll
    for (int ks = 0; ks < NS; ++ks) {
        const float* src = slab + ((size_t)ks * S_LEN + row) * DM + l * 8;
        a0 += *(const f32x4*)src; a1 += *(const f32x4*)(src + 4);
    }
    const v4u xr = *(const v4u*)(res + (size_t)row * DM + l * 8);
    float v[8];
#pragma unroll
    for (int i = 0; i < 4; ++i) {
        const float lo = i < 2 ? a0[2 * i] : a1[2 * i - 4], hi = i < 2 ? a0[2 * i + 1] : a1[2 * i - 3];
        v[2 * i] = rbf(__uint_as_float(xr[i] << 16) + rbf(lo)); v[2 * i + 1] = rbf(__uint_as_float(xr[i] & 0xffff0000u) + rbf(hi));
    }
    *(v4u*)(hout + (size_t)row * DM + l * 8) = (v4u){pk2(v[0], v[1]), pk2(v[2], v[3]), pk2(v[4], v[5]), pk2(v[6], v[7])};
    if (LN) {
        float sm = 0.f;
#pragma unroll
        for (int i = 0; i < 8; ++i) sm += v[i];
#pragma unroll
        for (int o = 32; o >= 1; o >>= 1) sm += __shfl_xor(sm, o);
        const float mean = sm * (1.0f / DM);
        float q = 0.f;
#pragma unroll
        for (int i = 0; i < 8; ++i) { const float d = v[i] - mean; q += d * d; }
#pragma unroll
        for (int o = 32; o >= 1; o >>= 1) q += __shfl_xor(q, o);
        const float mb = rbf(mean), den = rbf(sqrtf(rbf(rbf(q * (1.0f / DM)) + 1e-5f)));
        v4u o;
#pragma unroll
        for (int i = 0; i < 4; ++i) o[i] = pk2(rbf(v[2 * i] - mb) / den, rbf(v[2 * i + 1] - mb) / den);
        *(v4u*)(nout + (size_t)row * DM + l * 8) = o;
    }
}

constexpr unsigned SENT = 0xFFFFFFFFu;
constexpr int C_WV = 0;
constexpr int C_MEM = 131072;
constexpr int RST = 544;
constexpr int C_QS = C_MEM + 8192;
constexpr int C_KS = C_QS + 256;
constexpr int C_VS = C_QS + 8 * RST;
constexpr int C_KX = C_VS + 2048;
constexpr int C_VX = C_KX + 256;
constexpr int C_LDS = C_KX + 32 * RST;
constexpr unsigned SPIN_CAP = 1u << 22;
#define DLY1 14
#define DLY2 12

__device__ __forceinline__ __amdgpu_buffer_rsrc_t mk_rsrc(const void* p, unsigned bytes) {
    return __builtin_amdgcn_make_buffer_rsrc((void*)p, 0, bytes, 0x00020000);
}
__device__ __forceinline__ v4u ld16_sc1(__amdgpu_buffer_rsrc_t r, unsigned off) { return __builtin_amdgcn_raw_buffer_load_b128(r, off, 0, 16); }
__device__ __forceinline__ void st16_sc1(v4u v, __amdgpu_buffer_rsrc_t r, unsigned off) { __builtin_amdgcn_raw_buffer_store_b128(v, r, off, 0, 16); }
__device__ __forceinline__ void st4_sc1(unsigned v, __amdgpu_buffer_rsrc_t r, unsigned off) { __builtin_amdgcn_raw_buffer_store_b32(v, r, off, 0, 16); }
__device__ __forceinline__ void dly(int n) { for (int i = 0; i < n; ++i) __builtin_amdgcn_s_sleep(1); }

__device__ __forceinline__ f32x4 comb2(f32x4 a, f32x4 b) {
    f32x4 o;
#pragma unroll
    for (int r = 0; r < 4; ++r) {
        auto sw = __builtin_amdgcn_permlane32_swap(__float_as_uint(a[r]), __float_as_uint(b[r]), false, false);
        o[r] = __uint_as_float(sw[0]) + __uint_as_float(sw[1]);
    }
    return o;
}

#define STAMP(i)
__device__ __forceinline__ unsigned ld_flag(const unsigned* p) { return __hip_atomic_load(p, __ATOMIC_RELAXED, __HIP_MEMORY_SCOPE_AGENT); }
__device__ __forceinline__ void wait_flag_ge(const unsigned* p, unsigned want, int sleep_n) {
    unsigned spins = 0;
    while ((unsigned)__builtin_amdgcn_readfirstlane(ld_flag(p)) < want) { dly(sleep_n); if (++spins > SPIN_CAP) break; }
}
__device__ __forceinline__ void wait_flag_kv(const unsigned* p) {
    unsigned spins = 0;
    while (((unsigned)__builtin_amdgcn_readfirstlane(ld_flag(p)) & 3u) != 2u) { dly(2); if (++spins > SPIN_CAP) break; }
}
__device__ __forceinline__ void chain_body(unsigned char* smem, const u16* __restrict__ Wq, const u16* __restrict__ Wk, const u16* __restrict__ Wv,
                                           const u16* __restrict__ Wo, const u16* __restrict__ mem0,
                                           const u16* Kx, const u16* Vx, u16* kmem, u16* vmem, unsigned char* ws_sync) {
    LAS unsigned char* L = (LAS unsigned char*)smem;
    unsigned* flags = (unsigned*)(ws_sync + WS_FLAGS);
    const auto rK = mk_rsrc(Kx, (unsigned)S_LEN * HD * 2);
    const auto rV = mk_rsrc(Vx, (unsigned)S_LEN * HD * 2);
    const auto rKM = mk_rsrc(kmem, (unsigned)NSEG * NH * MM * DH * 2);
    const auto rVM = mk_rsrc(vmem, (unsigned)NSEG * NH * MM * DH * 2);
    const int h = blockIdx.x, t = threadIdx.x, w = __builtin_amdgcn_readfirstlane(t >> 6), l = t & 63, g = l >> 4, c16 = l & 15;
    const auto rRS = mk_rsrc(ws_sync + WS_RS, 2u * 32 * 32 * 512);
    const auto rAG = mk_rsrc(ws_sync + WS_AG, 2u * 8192);
    const int sidx = (c16 < 8) ? 0x4444 : 0xEEEE;

    bf16x16 Bq[2][8], Bk[2][8], Bo[8][2];
    {
        constexpr int ST = C_MEM;
        v4u P[4][4];
        auto gl = [&](int r, v4u (&d)[4]) {
#pragma unroll
            for (int i = 0; i < 4; ++i) {
                const int u = i * 256 + t, k = u >> 4, ch = u & 15;
                if (r < 24) {
                    const u16* W = r < 8 ? Wq : (r < 16 ? Wk : Wv);
                    d[i] = *(const v4u*)(W + (size_t)(64 * (r & 7) + k) * HD + h * DH + ch * 8);
                } else {
                    const int kk = (r - 24) >> 2, cb = (r - 24) & 3;
                    const int gg = k >> 4, wi = k & 15, ii = 2 * (wi >> 2) + (wi & 1), cc = (wi >> 1) & 1;
                    const int e = 64 * kk + 32 * cc + 8 * gg + ii;
                    d[i] = *(const v4u*)(Wo + ((size_t)h * DH + e) * DM + cb * 128 + ch * 8);
                }
            }
        };
#pragma unroll
        for (int r = 0; r < 4; ++r) gl(r, P[r]);
#pragma unroll
        for (int r = 0; r < 32; ++r) {
            __syncthreads();
#pragma unroll
            for (int i = 0; i < 4; ++i) { const int u = i * 256 + t; *(LAS v4u*)(L + ST + (u >> 4) * 288 + (u & 15) * 16) = P[r & 3][i]; }
            if (r + 4 < 32) gl(r + 4, P[r & 3]);
            __syncthreads();
            if (r < 24) {
                const int mat = r >> 3, ks = r & 7;
#pragma unroll
                for (int tt = 0; tt < 2; ++tt) {
                    const LAS unsigned char* p = L + ST + (8 * g + (c16 >> 2)) * 288 + (32 * w + 16 * tt + (c16 & 3) * 4) * 2;
                    const bf16x8 lo = cat8(tr_read(p), tr_read(p + 4 * 288));
                    const bf16x8 hi = cat8(tr_read(p + 32 * 288), tr_read(p + 36 * 288));
                    if (mat == 0) Bq[tt][ks] = cat16(lo, hi);
                    else if (mat == 1) Bk[tt][ks] = cat16(lo, hi);
                    else {
                        *(LAS bf16x8*)(L + C_WV + (((2 * w + tt) * 8 + ks) * 2 + 0) * 1024 + l * 16) = lo;
                        *(LAS bf16x8*)(L + C_WV + (((2 * w + tt) * 8 + ks) * 2 + 1) * 1024 + l * 16) = hi;
                    }
                }
            } else {
                const int kk = (r - 24) >> 2, cb = (r - 24) & 3;
                if (w == cb) {
#pragma unroll
                    for (int tt = 0; tt < 8; ++tt) {
                        const LAS unsigned char* p = L + ST + (8 * g + (c16 >> 2)) * 288 + (16 * tt + (c16 & 3) * 4) * 2;
                        const bf16x8 lo = cat8(tr_read(p), tr_read(p + 4 * 288));
                        const bf16x8 hi = cat8(tr_read(p + 32 * 288), tr_read(p + 36 * 288));
                        Bo[tt][kk] = cat16(lo, hi);
                    }
                }
            }
        }
        __syncthreads();
    }
    {
#pragma unroll
        for (int i = 0; i < 2; ++i) {
            const int u = i * 256 + t, ks = u >> 6, ll = u & 63, gg = ll >> 4, cc = (ll >> 3) & 1, m = ll & 7;
            const u16* src = mem0 + m * DM + 64 * ks + 16 * gg + 2 * cc;
            v4u v;
#pragma unroll
            for (int q = 0; q < 4; ++q) v[q] = *(const unsigned*)(src + 4 * q);
            *(LAS v4u*)(L + C_MEM + u * 16) = v;
        }
        wait_flag_kv(flags + FL_QKV + h * 16);
#pragma unroll
        for (int i = 0; i < 2; ++i) {
            const int u = i * 256 + t, r = u >> 4, ch = u & 15;
            *(LAS v4u*)(L + C_KX + r * RST + ch * 16) = ld16_sc1(rK, (unsigned)(r * HD + h * DH + ch * 8) * 2u);
            *(LAS v4u*)(L + C_VX + r * RST + ch * 16) = ld16_sc1(rV, (unsigned)(r * HD + h * DH + ch * 8) * 2u);
        }
    }
    __syncthreads();

#define PIN(x) asm volatile("" : "+v"(x))
    for (int s = 0; s < NSEG; ++s) {
        int t = threadIdx.x; PIN(t);
        const int l = t & 63, g = l >> 4, c16 = l & 15;
        unsigned oWV = (unsigned)(size_t)(L + C_WV + (2 * w * 16) * 1024 + l * 16);
        unsigned oMEM = (unsigned)(size_t)(L + C_MEM + l * 16);
        unsigned oKX = (unsigned)(size_t)(L + C_KX + c16 * RST + 16 * g);
        unsigned oQK = (unsigned)(size_t)(L + C_QS + (c16 & 7) * RST + 16 * g);
        unsigned oQW = (unsigned)(size_t)(L + C_QS + 4 * (g & 1) * RST + (32 * w + 16 * (l >> 5) + c16) * 2);
        unsigned oVW = (unsigned)(size_t)(L + C_VS + (32 * w + 16 * (l >> 5) + c16) * 16 + (g & 1) * 8);
        PIN(oWV); PIN(oMEM); PIN(oKX); PIN(oQK); PIN(oQW); PIN(oVW);
#define LP(o) ((LAS unsigned char*)(size_t)(o))
        f32x4 aq[2], ak[2], av[2];
#pragma unroll
        for (int i = 0; i < 2; ++i) { aq[i] = (f32x4){0.f, 0.f, 0.f, 0.f}; ak[i] = aq[i]; av[i] = aq[i]; PIN(aq[i]); PIN(ak[i]); PIN(av[i]); }
        bf16x8 A[8], Vb[2][4];
#pragma unroll
        for (int i = 0; i < 4; ++i) Vb[0][i] = *(const LAS bf16x8*)(LP(oWV) + ((i >> 1) * 16 + (i & 1)) * 1024);
        if (s > 0) {
            const unsigned slot = (s - 1) & 1;
            dly(DLY2);
            v4u x[2];
            unsigned spins = 0;
            for (;;) {
                asm volatile("" ::: "memory");
                bool ok = true;
#pragma unroll
                for (int i = 0; i < 2; ++i) x[i] = ld16_sc1(rAG, slot * 8192u + (i * 256 + t) * 16u);
#pragma unroll
                for (int i = 0; i < 2; ++i) ok &= (x[i][0] != SENT) & (x[i][1] != SENT) & (x[i][2] != SENT) & (x[i][3] != SENT);
                if (__all(ok)) break;
                if (++spins > SPIN_CAP) break;
            }
#pragma unroll
            for (int i = 0; i < 2; ++i) *(LAS v4u*)(LP(oMEM) + (i * 256 + w * 64) * 16) = x[i];
            STAMP(6);
            __syncthreads();
            STAMP(7);
            if (t == 0) __hip_atomic_fetch_add(flags + FL_PROG, 1u, __ATOMIC_RELAXED, __HIP_MEMORY_SCOPE_AGENT);
        }
#pragma unroll
        for (int ks = 0; ks < 2; ++ks) A[ks] = *(const LAS bf16x8*)(LP(oMEM) + ks * 1024);
        SBAR();
#pragma unroll
        for (int ks = 0; ks < 8; ++ks) {
            if (ks < 6) A[ks + 2] = *(const LAS bf16x8*)(LP(oMEM) + (ks + 2) * 1024);
            if (ks + 1 < 8) {
#pragma unroll
                for (int i = 0; i < 4; ++i) Vb[(ks + 1) & 1][i] = *(const LAS bf16x8*)(LP(oWV) + ((i >> 1) * 16 + (ks + 1) * 2 + (i & 1)) * 1024);
            }
            SBAR();
            aq[0] = SMFMAC(A[ks], Bq[0][ks], aq[0], sidx);
            aq[1] = SMFMAC(A[ks], Bq[1][ks], aq[1], sidx);
            ak[0] = SMFMAC(A[ks], Bk[0][ks], ak[0], sidx);
            ak[1] = SMFMAC(A[ks], Bk[1][ks], ak[1], sidx);
            av[0] = SMFMAC(A[ks], cat16(Vb[ks & 1][0], Vb[ks & 1][1]), av[0], sidx);
            av[1] = SMFMAC(A[ks], cat16(Vb[ks & 1][2], Vb[ks & 1][3]), av[1], sidx);
            SBAR();
        }
        bf16x8 kx[2][4];
#pragma unroll
        for (int kq = 0; kq < 4; ++kq) {
            kx[0][kq] = *(const LAS bf16x8*)(LP(oKX) + 64 * kq);
            kx[1][kq] = *(const LAS bf16x8*)(LP(oKX) + 16 * RST + 64 * kq);
        }
        {
            const f32x4 q = comb2(aq[0], aq[1]), k = comb2(ak[0], ak[1]), v = comb2(av[0], av[1]);
            const unsigned q01 = pk2(q[0], q[1]), q23 = pk2(q[2], q[3]), k01 = pk2(k[0], k[1]), k23 = pk2(k[2], k[3]);
            LAS unsigned char* pq = LP(oQW);
            LAS unsigned char* pk = LP(oQW) + (C_KS - C_QS);
            *(LAS u16*)(pq) = (u16)q01; *(LAS u16*)(pq + RST) = (u16)(q01 >> 16); *(LAS u16*)(pq + 2 * RST) = (u16)q23; *(LAS u16*)(pq + 3 * RST) = (u16)(q23 >> 16);
            *(LAS u16*)(pk) = (u16)k01; *(LAS u16*)(pk + RST) = (u16)(k01 >> 16); *(LAS u16*)(pk + 2 * RST) = (u16)k23; *(LAS u16*)(pk + 3 * RST) = (u16)(k23 >> 16);
            v2u vv = {pk2(v[0], v[1]), pk2(v[2], v[3])};
            *(LAS v2u*)(LP(oVW)) = vv;
        }
        unsigned oVX = (unsigned)(size_t)(L + C_VX + (4 * g + (c16 >> 2)) * RST + (c16 & 3) * 8 + 64 * w);
        unsigned oVM = (unsigned)(size_t)(L + C_VS + c16 * 16 + (g & 1) * 8 + 512 * w);
        unsigned oOW = (unsigned)(size_t)(L + C_MEM + (c16 & 7) * RST + (32 * w + 4 * g) * 2);
        unsigned oOR = (unsigned)(size_t)(L + C_MEM + (c16 & 7) * RST + (32 * (c16 >> 3) + 8 * g) * 2);
        PIN(oVX); PIN(oVM); PIN(oOW); PIN(oOR);
        f32x4 oo[8];
        STAMP(0);
        __syncthreads();
        STAMP(1);
        if (s == NSEG - 1) break;
        bf16x8 aO[2];
        {
            bf16x8 qb[4], km_[4];
#pragma unroll
            for (int kq = 0; kq < 4; ++kq) {
                qb[kq] = *(const LAS bf16x8*)(LP(oQK) + 64 * kq);
                km_[kq] = *(const LAS bf16x8*)(LP(oQK) + (C_KS - C_QS) + 64 * kq);
            }
            bf16x8 va[2]; v2u vm[2];
#pragma unroll
            for (int T = 0; T < 2; ++T) {
                va[T] = cat8(tr_read(LP(oVX) + 32 * T), tr_read(LP(oVX) + 32 * T + 16 * RST));
                vm[T] = *(const LAS v2u*)(LP(oVM) + 256 * T);
            }
            SBAR();
            f32x4 sc[3];
#pragma unroll
            for (int T = 0; T < 3; ++T) sc[T] = (f32x4){0.f, 0.f, 0.f, 0.f};
#pragma unroll
            for (int kq = 0; kq < 4; ++kq) {
                sc[0] = MFMA32(kx[0][kq], qb[kq], sc[0]); sc[1] = MFMA32(kx[1][kq], qb[kq], sc[1]); sc[2] = MFMA32(km_[kq], qb[kq], sc[2]);
            }
            SBAR();
            const float cs = 0.08838834764831845f * 1.4426950408889634f;
            if (g >= 2) sc[2] = (f32x4){-3.0e38f, -3.0e38f, -3.0e38f, -3.0e38f};
            float mx = fmaxf(fmaxf(fmaxf(sc[0][0], sc[0][1]), fmaxf(sc[0][2], sc[0][3])), fmaxf(fmaxf(sc[1][0], sc[1][1]), fmaxf(sc[1][2], sc[1][3])));
            mx = fmaxf(mx, fmaxf(fmaxf(sc[2][0], sc[2][1]), fmaxf(sc[2][2], sc[2][3])));
            mx = xl32_max(xl16_max(mx));
            const float nm = -mx * cs;
            float p[12];
#pragma unroll
            for (int T = 0; T < 3; ++T)
#pragma unroll
                for (int r = 0; r < 4; ++r) p[T * 4 + r] = __builtin_amdgcn_exp2f(__builtin_fmaf(sc[T][r], cs, nm));
            float sum = ((p[0] + p[1]) + (p[2] + p[3])) + ((p[4] + p[5]) + (p[6] + p[7])) + ((p[8] + p[9]) + (p[10] + p[11]));
            sum = xl32_add(xl16_add(sum));
            const float inv = __builtin_amdgcn_rcpf(sum);
            v4u px = {pk2(p[0] * inv, p[1] * inv), pk2(p[2] * inv, p[3] * inv), pk2(p[4] * inv, p[5] * inv), pk2(p[6] * inv, p[7] * inv)};
            const bf16x8 Px = __builtin_bit_cast(bf16x8, px);
            const bf16x8 Pm = __builtin_bit_cast(bf16x8, (v4u){pk2(p[8] * inv, p[9] * inv), pk2(p[10] * inv, p[11] * inv), 0u, 0u});
            f32x4 o[2];
#pragma unroll
            for (int T = 0; T < 2; ++T) o[T] = MFMA32(va[T], Px, ((f32x4){0.f, 0.f, 0.f, 0.f}));
#pragma unroll
            for (int T = 0; T < 2; ++T) o[T] = MFMA32(__builtin_bit_cast(bf16x8, (v4u){vm[T][0], vm[T][1], 0u, 0u}), Pm, o[T]);
            if (c16 < 8) {
#pragma unroll
                for (int T = 0; T < 2; ++T) *(LAS v2u*)(LP(oOW) + 32 * T) = (v2u){pk2(o[T][0], o[T][1]), pk2(o[T][2], o[T][3])};
            }
#pragma unroll
            for (int tt = 0; tt < 8; ++tt) { oo[tt] = (f32x4){0.f, 0.f, 0.f, 0.f}; PIN(oo[tt]); }
            __syncthreads();
            aO[0] = *(const LAS bf16x8*)(LP(oOR));
            aO[1] = *(const LAS bf16x8*)(LP(oOR) + 128);
        }
        STAMP(2);
        const unsigned slot = s & 1, par = (s >> 1) & 1;
        {
#pragma unroll
            for (int tt = 0; tt < 8; ++tt) oo[tt] = SMFMAC(aO[0], Bo[tt][0], oo[tt], sidx);
#pragma unroll
            for (int tt = 0; tt < 8; ++tt) oo[tt] = SMFMAC(aO[1], Bo[tt][1], oo[tt], sidx);
#pragma unroll
            for (int tp = 0; tp < 4; ++tp) {
                const f32x4 o = comb2(oo[2 * tp], oo[2 * tp + 1]);
                v4u st;
#pragma unroll
                for (int r = 0; r < 4; ++r) st[r] = (__float_as_uint(o[r]) & ~1u) | par;
                const unsigned cons = 8 * w + 2 * tp + (l >> 5);
                st16_sc1(st, rRS, slot * 524288u + cons * 16384u + h * 512u + (l & 31) * 16u);
            }
        }
        STAMP(3);
        {
            const int u = t & 127;
            const v4u pub = *(const LAS v4u*)(L + (t < 128 ? C_KS + (u >> 4) * RST + (u & 15) * 16 : C_VS + u * 16));
            const unsigned off = (unsigned)((s * NH + h) * (MM * DH) + u * 8) * 2u;
            if (t < 128) st16_sc1(pub, rKM, off); else st16_sc1(pub, rVM, off);
        }
        if (((s + 1) & 3) == 0) wait_flag_kv(flags + FL_QKV + (((s + 1) >> 2) * 32 + h) * 16);
        v4u pk_[2], pv_[2];
#pragma unroll
        for (int i = 0; i < 2; ++i) {
            const int u = i * 256 + t, r = u >> 4, ch = u & 15;
            pk_[i] = ld16_sc1(rK, (unsigned)(((s + 1) * SEG + r) * HD + h * DH + ch * 8) * 2u);
            pv_[i] = ld16_sc1(rV, (unsigned)(((s + 1) * SEG + r) * HD + h * DH + ch * 8) * 2u);
        }
        const int lr = (l >> 3) & 3;
        unsigned offAG;
        {
            const int m = 4 * (w >> 1) + lr, cp = 4 * (w & 1) + ((l & 7) >> 1);
            offAG = slot * 8192u + (h >> 2) * 1024u + ((h & 3) * 16 + (cp & 1) * 8 + m) * 16u + (cp >> 1) * 4u;
            PIN(offAG);
        }
        dly(DLY1);
        {
            v4u x[4];
            const unsigned base = slot * 524288u + h * 16384u + (l >> 3) * 512u + (8 * w + (l & 7)) * 16u;
            unsigned spins = 0;
            for (;;) {
                asm volatile("" ::: "memory");
                unsigned bad = 0;
#pragma unroll
                for (int i = 0; i < 4; ++i) x[i] = ld16_sc1(rRS, base + i * 4096u);
#pragma unroll
                for (int i = 0; i < 4; ++i) bad |= (x[i][0] ^ par) | (x[i][1] ^ par) | (x[i][2] ^ par) | (x[i][3] ^ par);
                if (__all((bad & 1u) == 0)) break;
                if (++spins > SPIN_CAP) break;
            }
            STAMP(4);
            float sm[4];
#pragma unroll
            for (int r = 0; r < 4; ++r) {
                float v = (__uint_as_float(x[0][r]) + __uint_as_float(x[1][r])) + (__uint_as_float(x[2][r]) + __uint_as_float(x[3][r]));
                sm[r] = xl32_add(xl16_add(xl8_add(v)));
            }
            const float own = lr == 0 ? sm[0] : (lr == 1 ? sm[1] : (lr == 2 ? sm[2] : sm[3]));
            const float nb = xl1_get(own);
            unsigned pkd = pk2(own, nb);
            if (pkd == SENT) pkd = 0x7FC07FC0u;
            if (l < 32 && !(l & 1)) {
                st4_sc1(pkd, rAG, offAG);
                st4_sc1(SENT, rAG, offAG ^ 8192u);
            }
        }
        STAMP(5);
#pragma unroll
        for (int i = 0; i < 2; ++i) {
            const int u = i * 256 + t, r = u >> 4, ch = u & 15;
            *(LAS v4u*)(L + C_KX + r * RST + ch * 16) = pk_[i];
            *(LAS v4u*)(L + C_VX + r * RST + ch * 16) = pv_[i];
        }
    }
    {
        const int u = t & 127;
        const v4u pub = *(const LAS v4u*)(L + (t < 128 ? C_KS + (u >> 4) * RST + (u & 15) * 16 : C_VS + u * 16));
        const unsigned off = (unsigned)(((NSEG - 1) * NH + h) * (MM * DH) + u * 8) * 2u;
        if (t < 128) st16_sc1(pub, rKM, off); else st16_sc1(pub, rVM, off);
        asm volatile("s_waitcnt vmcnt(0)" ::: "memory");
        __syncthreads();
        if (t == 0) __hip_atomic_fetch_add(flags + FL_PROG, 1u, __ATOMIC_RELAXED, __HIP_MEMORY_SCOPE_AGENT);
    }
}

__device__ __forceinline__ void xattn_task(unsigned char* smem, const u16* Q, const u16* Kx, const u16* Vx,
                                           const u16* kmem, const u16* vmem, u16* __restrict__ O, int s, int h) {
    const int t = threadIdx.x, w = t >> 6, l = t & 63, g = l >> 4, c16 = l & 15;
    LAS unsigned char* LV = (LAS unsigned char*)smem + w * 32 * 288;
    const size_t rowb = (size_t)s * SEG;
#pragma unroll
    for (int i = 0; i < 8; ++i) {
        const int u = i * 64 + l, r = u >> 4, ch = u & 15;
        *(LAS v4u*)(LV + r * 288 + ch * 16) = *(const v4u*)(Vx + (rowb + r) * HD + h * DH + ch * 8);
    }
    f32x4 sc[2][3];
#pragma unroll
    for (int qt = 0; qt < 2; ++qt)
#pragma unroll
        for (int T = 0; T < 3; ++T) sc[qt][T] = (f32x4){0.f, 0.f, 0.f, 0.f};
#pragma unroll
    for (int kq = 0; kq < 4; ++kq) {
        const int eo = 32 * kq + 8 * g;
        const bf16x8 q0 = *(const bf16x8*)(Q + (rowb + c16) * HD + h * DH + eo);
        const bf16x8 q1 = *(const bf16x8*)(Q + (rowb + 16 + c16) * HD + h * DH + eo);
        const bf16x8 k0 = *(const bf16x8*)(Kx + (rowb + c16) * HD + h * DH + eo);
        const bf16x8 k1 = *(const bf16x8*)(Kx + (rowb + 16 + c16) * HD + h * DH + eo);
        const bf16x8 k2 = *(const bf16x8*)(kmem + ((size_t)(s * NH + h) * MM + (c16 & 7)) * DH + eo);
        sc[0][0] = MFMA32(k0, q0, sc[0][0]); sc[0][1] = MFMA32(k1, q0, sc[0][1]); sc[0][2] = MFMA32(k2, q0, sc[0][2]);
        sc[1][0] = MFMA32(k0, q1, sc[1][0]); sc[1][1] = MFMA32(k1, q1, sc[1][1]); sc[1][2] = MFMA32(k2, q1, sc[1][2]);
    }
    bf16x8 Px[2], Pm[2];
    const float scale = 0.08838834764831845f;
#pragma unroll
    for (int qt = 0; qt < 2; ++qt) {
        const int qi = 16 * qt + c16;
        float p[12]; float mx = -3.0e38f;
#pragma unroll
        for (int T = 0; T < 3; ++T)
#pragma unroll
            for (int r = 0; r < 4; ++r) {
                float v = rbf(rbf(sc[qt][T][r]) * scale);
                const bool valid = (T == 2) ? (g < 2) : (16 * T + 4 * g + r <= qi);
                if (!valid) v = -3.0e38f;
                p[T * 4 + r] = v; mx = fmaxf(mx, v);
            }
        mx = fmaxf(mx, __shfl_xor(mx, 16)); mx = fmaxf(mx, __shfl_xor(mx, 32));
        float sum = 0.f;
#pragma unroll
        for (int i = 0; i < 12; ++i) { p[i] = (p[i] < -1.0e38f) ? 0.f : __expf(p[i] - mx); sum += p[i]; }
        sum += __shfl_xor(sum, 16); sum += __shfl_xor(sum, 32);
        const float inv = 1.0f / sum;
        v4u px = {pk2(p[0] * inv, p[1] * inv), pk2(p[2] * inv, p[3] * inv), pk2(p[4] * inv, p[5] * inv), pk2(p[6] * inv, p[7] * inv)};
        v2u pm = {pk2(p[8] * inv, p[9] * inv), pk2(p[10] * inv, p[11] * inv)};
        Px[qt] = __builtin_bit_cast(bf16x8, px); Pm[qt] = __builtin_bit_cast(bf16x8, (v4u){pm[0], pm[1], 0u, 0u});
    }
    __syncthreads();
#pragma unroll
    for (int T = 0; T < 8; ++T) {
        const LAS unsigned char* pv = LV + (4 * g + (c16 >> 2)) * 288 + (16 * T + (c16 & 3) * 4) * 2;
        const bf16x8 va = cat8(tr_read(pv), tr_read(pv + 16 * 288));
        v2u vm = *(const v2u*)(vmem + (size_t)(s * NH + h) * (MM * DH) + (16 * T + c16) * 8 + (g & 1) * 4);
        if (g >= 2) vm = (v2u){0u, 0u};
#pragma unroll
        for (int qt = 0; qt < 2; ++qt) {
            f32x4 o = (f32x4){0.f, 0.f, 0.f, 0.f};
            o = MFMA32(va, Px[qt], o);
            o = MFMA32(__builtin_bit_cast(bf16x8, (v4u){vm[0], vm[1], 0u, 0u}), Pm[qt], o);
            v2u st = {pk2(o[0], o[1]), pk2(o[2], o[3])};
            st8_dev(O + (rowb + 16 * qt + c16) * HD + h * DH + 16 * T + 4 * g, st);
        }
    }
}

constexpr int NBULK = 224;
#define NRB_IN 12
struct Params {
    const u16 *x, *mem0, *Wq, *Wk, *Wv, *Wo, *wg, *wu, *wd;
    u16* out; unsigned char* ws;
};
__device__ __forceinline__ void bulk_wait(const unsigned* p, unsigned want) {
    if (threadIdx.x < 64) {
        unsigned spins = 0;
        while ((unsigned)__builtin_amdgcn_readfirstlane(ld_flag(p)) < want) { __builtin_amdgcn_s_sleep(48); if (++spins > SPIN_CAP) break; }
    }
    __syncthreads();
    __builtin_amdgcn_fence(__ATOMIC_ACQUIRE, "agent");
    asm volatile("s_waitcnt vmcnt(0)" ::: "memory");
}
__device__ __forceinline__ void bulk_arrive(unsigned* p) {
    asm volatile("s_waitcnt vmcnt(0)" ::: "memory");
    __syncthreads();
    if (threadIdx.x == 0) __hip_atomic_fetch_add(p, 1u, __ATOMIC_RELAXED, __HIP_MEMORY_SCOPE_AGENT);
}
__global__ void __launch_bounds__(256, 1) k_fused(Params P) {
    extern __shared__ __attribute__((aligned(16))) unsigned char smem[];
    unsigned char* ws = P.ws;
    u16* xn = (u16*)(ws + WS_XN); u16* Q = (u16*)(ws + WS_Q); u16* Kx = (u16*)(ws + WS_K); u16* Vx = (u16*)(ws + WS_V);
    u16* O = (u16*)(ws + WS_O); u16* kmem = (u16*)(ws + WS_KM); u16* vmem = (u16*)(ws + WS_VM);
    if (blockIdx.x < NH) {
        chain_body(smem, P.Wq, P.Wk, P.Wv, P.Wo, P.mem0, Kx, Vx, kmem, vmem, ws);
        return;
    }
    u16* H = (u16*)(ws + WS_H); u16* HN = (u16*)(ws + WS_HN); u16* ACT = (u16*)(ws + WS_ACT);
    unsigned* flags = (unsigned*)(ws + WS_FLAGS);
    const int b = blockIdx.x - NH, t = threadIdx.x, w = __builtin_amdgcn_readfirstlane(t >> 6), l = t & 63;
    for (int i = b; i < 16 * 96; i += NBULK) {
        int rb, hh, mat;
        if (i < 1024) { rb = i >> 6; hh = (i >> 1) & 31; mat = 1 + (i & 1); }
        else { const int j = i - 1024; rb = j >> 5; hh = j & 31; mat = 0; }
        const u16* W = mat == 0 ? P.Wq : (mat == 1 ? P.Wk : P.Wv);
        u16* C = mat == 0 ? Q : (mat == 1 ? Kx : Vx);
        gemm_tile<0, true, 128, 128>(smem, xn, DM, W, nullptr, HD, C, HD, nullptr, DM, rb * 128, hh * DH);
        asm volatile("s_waitcnt vmcnt(0)" ::: "memory");
        __syncthreads();
        if (t == 0) __hip_atomic_fetch_add(flags + FL_QKV + (rb * 32 + hh) * 16, mat == 0 ? 4u : 1u, __ATOMIC_RELAXED, __HIP_MEMORY_SCOPE_AGENT);
    }
    float* slab = (float*)(ws + WS_SLAB);
    const int role = b < 48 ? 0 : (b < 112 ? 1 : (b < 128 ? 2 : (b < 192 ? 3 : 4)));
    const int rb0 = role == 0 ? 0 : (role == 1 ? 48 : (role == 2 ? 112 : (role == 3 ? 128 : 192)));
    const int nrole = role == 0 ? 48 : (role == 1 ? 64 : (role == 2 ? 16 : (role == 3 ? 64 : 32)));
    const int ntask = role == 0 ? 32 : (role == 1 ? 16 : (role == 2 ? 16 : (role == 3 ? 28 : 8)));
    const int nrb = role == 0 ? 16 : NRB_IN;
    for (int idx = b - rb0; idx < nrb * ntask; idx += nrole) {
        const int rb = idx / ntask, r = idx % ntask, m0 = rb * 128;
        unsigned* gf = flags + FL_GRP + rb * 4 * 16;
        if (role == 0) {
            const int s = 4 * rb + (r >> 3), hh = (r & 7) * 4 + w;
            wait_flag_ge(flags + FL_QKV + (rb * 32 + hh) * 16, 6, 32);
            bulk_wait(flags + FL_PROG, (unsigned)(s + 1) * NH);
            xattn_task(smem, Q, Kx, Vx, kmem, vmem, O, s, hh);
            bulk_arrive(gf + 0 * 16);
        } else if (role == 1) {
            const int ks = r >> 2, nt = r & 3;
            bulk_wait(gf + 0 * 16, 32);
            gemm_tile<4, true, 128, 128>(smem, O + ks * 1024, HD, P.Wo + (size_t)ks * 1024 * DM, nullptr, DM, (u16*)(slab + (size_t)ks * S_LEN * DM), DM, nullptr, 1024, m0, nt * 128);
            bulk_arrive(gf + 1 * 16);
        } else if (role == 2) {
            bulk_wait(gf + 1 * 16, 16);
#pragma unroll
            for (int rr = 0; rr < 2; ++rr) {
                const int row = m0 + r * 8 + w * 2 + rr;
                f32x4 a0 = (f32x4){0.f, 0.f, 0.f, 0.f}, a1 = a0;
#pragma unroll
                for (int ks = 0; ks < 4; ++ks) {
                    const float* src = slab + ((size_t)ks * S_LEN + row) * DM + l * 8;
                    a0 += *(const f32x4*)src; a1 += *(const f32x4*)(src + 4);
                }
                const v4u xr = *(const v4u*)(P.x + (size_t)row * DM + l * 8);
                float v[8];
#pragma unroll
                for (int i = 0; i < 4; ++i) {
                    const float lo = i < 2 ? a0[2 * i] : a1[2 * i - 4], hi = i < 2 ? a0[2 * i + 1] : a1[2 * i - 3];
                    v[2 * i] = rbf(__uint_as_float(xr[i] << 16) + rbf(lo)); v[2 * i + 1] = rbf(__uint_as_float(xr[i] & 0xffff0000u) + rbf(hi));
                }
                float sm = 0.f;
#pragma unroll
                for (int i = 0; i < 8; ++i) sm += v[i];
#pragma unroll
                for (int o = 32; o >= 1; o >>= 1) sm += __shfl_xor(sm, o);
                const float mean = sm * (1.0f / DM);
                float q = 0.f;
#pragma unroll
                for (int i = 0; i < 8; ++i) { const float d = v[i] - mean; q += d * d; }
#pragma unroll
                for (int o = 32; o >= 1; o >>= 1) q += __shfl_xor(q, o);
                const float mb = rbf(mean), den = rbf(sqrtf(rbf(rbf(q * (1.0f / DM)) + 1e-5f)));
                v2u h0 = {pk2(v[0], v[1]), pk2(v[2], v[3])}, h1 = {pk2(v[4], v[5]), pk2(v[6], v[7])};
                v2u n0 = {pk2(rbf(v[0] - mb) / den, rbf(v[1] - mb) / den), pk2(rbf(v[2] - mb) / den, rbf(v[3] - mb) / den)};
                v2u n1 = {pk2(rbf(v[4] - mb) / den, rbf(v[5] - mb) / den), pk2(rbf(v[6] - mb) / den, rbf(v[7] - mb) / den)};
                st8_dev(H + (size_t)row * DM + l * 8, h0); st8_dev(H + (size_t)row * DM + l * 8 + 4, h1);
                st8_dev(HN + (size_t)row * DM + l * 8, n0); st8_dev(HN + (size_t)row * DM + l * 8 + 4, n1);
            }
            bulk_arrive(gf + 2 * 16);
        } else if (role == 3) {
            bulk_wait(gf + 2 * 16, 16);
            gemm_tile<2, true, 128, 128>(smem, HN, DM, P.wg, P.wu, FF, ACT, FF, nullptr, DM, m0, r * 64);
            bulk_arrive(gf + 3 * 16);
        } else {
            bulk_wait(gf + 3 * 16, 28);
            gemm_tile<1, false, 128, 64>(smem, ACT, FF, P.wd, nullptr, DM, P.out, DM, H, FF, m0, r * 64);
        }
    }
}

extern "C" void kernel_launch(void* const* d_in, const int* in_sizes, int n_in, void* d_out, int out_size, void* d_ws, size_t ws_size,
                              hipStream_t stream) {
    const u16* x = (const u16*)d_in[0];
    const u16* mem0 = (const u16*)d_in[1];
    const u16* Wq = (const u16*)d_in[2];
    const u16* Wk = (const u16*)d_in[3];
    const u16* Wv = (const u16*)d_in[4];
    const u16* Wo = (const u16*)d_in[5];
    const u16* wg = (const u16*)d_in[8];
    const u16* wu = (const u16*)d_in[9];
    const u16* wd = (const u16*)d_in[10];
    u16* out = (u16*)d_out;
    unsigned char* ws = (unsigned char*)d_ws;
    u16* xn = (u16*)(ws + WS_XN); u16* Q = (u16*)(ws + WS_Q); u16* K = (u16*)(ws + WS_K); u16* V = (u16*)(ws + WS_V);
    u16* O = (u16*)(ws + WS_O); u16* km = (u16*)(ws + WS_KM); u16* vm = (u16*)(ws + WS_VM);
    u16* H = (u16*)(ws + WS_H); u16* HN = (u16*)(ws + WS_HN); u16* ACT = (u16*)(ws + WS_ACT);

    (void)hipFuncSetAttribute((const void*)k_fused, hipFuncAttributeMaxDynamicSharedMemorySize, C_LDS);
#define TBM 64
#define TBN 64
#define TBM1 64
    (void)hipFuncSetAttribute((const void*)k_gemm<2, TBM1, 128>, hipFuncAttributeMaxDynamicSharedMemorySize, 2 * g_stage(TBM1, 128));
    (void)hipFuncSetAttribute((const void*)k_gemm_splitk<TBM, TBN>, hipFuncAttributeMaxDynamicSharedMemorySize, 2 * g_stage(TBM, TBN));
    k_ln<true><<<S_LEN / 4, 256, 0, stream>>>(x, xn, ws);
    Params P{x, mem0, Wq, Wk, Wv, Wo, wg, wu, wd, out, ws};
    k_fused<<<NH + NBULK, 256, C_LDS, stream>>>(P);
    float* slab = (float*)(ws + WS_SLAB);
    constexpr int R0 = NRB_IN * 128, NR = S_LEN - R0;
    k_gemm_splitk<TBM, TBN><<<dim3(DM / TBN, NR / TBM, 8), 256, 2 * g_stage(TBM, TBN), stream>>>(O, HD, Wo, DM, slab, HD / 8, R0);
    k_reduce<8, true><<<NR / 4, 256, 0, stream>>>(slab, x, H, HN, R0);
    k_gemm<2, TBM1, 128><<<dim3(FF / 64, NR / TBM1), 256, 2 * g_stage(TBM1, 128), stream>>>(HN, DM, wg, wu, FF, ACT, FF, nullptr, DM, R0);
    k_gemm_splitk<TBM, TBN><<<dim3(DM / TBN, NR / TBM, 7), 256, 2 * g_stage(TBM, TBN), stream>>>(ACT, FF, wd, DM, slab, FF / 7, R0);
    k_reduce<7, false><<<NR / 4, 256, 0, stream>>>(slab, H, out, nullptr, R0);
}
```

```cpp
#include <hip/hip_runtime.h>
#include <hip/hip_bf16.h>
#include <stdint.h>

typedef unsigned short u16;
typedef __bf16 bf16x8 __attribute__((ext_vector_type(8)));
typedef __bf16 bf16x4 __attribute__((ext_vector_type(4)));
typedef __bf16 bf16x16 __attribute__((ext_vector_type(16)));
typedef short s16x4 __attribute__((ext_vector_type(4)));
typedef float f32x4 __attribute__((ext_vector_type(4)));
typedef unsigned v4u __attribute__((ext_vector_type(4)));
typedef unsigned v2u __attribute__((ext_vector_type(2)));
#define LAS __attribute__((address_space(3)))

constexpr int S_LEN = 2048, DM = 512, NH = 32, DH = 128, HD = NH * DH, MM = 8, SEG = 32, NSEG = S_LEN / SEG, FF = 1792;

constexpr size_t WS_RS = 0;
constexpr size_t WS_AG = WS_RS + 2u * 32 * 32 * 512;
constexpr size_t WS_SYNC_END = WS_AG + 2u * 8192;
constexpr size_t WS_FLAGS = WS_SYNC_END;
constexpr int FL_QKV = 0;
constexpr int FL_PROG = 16 * 32 * 16;
constexpr int FL_GRP = FL_PROG + 64;
constexpr int FL_WORDS = FL_GRP + 16 * 4 * 16;
constexpr size_t WS_FLAGS_END = WS_FLAGS + FL_WORDS * 4;
constexpr size_t WS_XN = 2u << 20;
constexpr size_t WS_Q = WS_XN + (size_t)S_LEN * DM * 2;
constexpr size_t WS_K = WS_Q + (size_t)S_LEN * HD * 2;
constexpr size_t WS_V = WS_K + (size_t)S_LEN * HD * 2;
constexpr size_t WS_O = WS_V + (size_t)S_LEN * HD * 2;
constexpr size_t WS_KM = WS_O + (size_t)S_LEN * HD * 2;
constexpr size_t WS_VM = WS_KM + (size_t)NSEG * NH * MM * DH * 2;
constexpr size_t WS_H = WS_VM + (size_t)NSEG * NH * MM * DH * 2;
constexpr size_t WS_HN = WS_H + (size_t)S_LEN * DM * 2;
constexpr size_t WS_ACT = WS_HN + (size_t)S_LEN * DM * 2;
constexpr size_t WS_SLAB = WS_ACT + (size_t)S_LEN * FF * 2;
constexpr size_t WS_END = WS_SLAB + (size_t)8 * S_LEN * DM * 4;

__device__ __forceinline__ float bf2f(u16 v) { return __uint_as_float((unsigned)v << 16); }
__device__ __forceinline__ u16 f2bf(float f) { return __builtin_bit_cast(u16, (__bf16)f); }
__device__ __forceinline__ float rbf(float f) { return (float)(__bf16)f; }
typedef __bf16 bf16x2 __attribute__((ext_vector_type(2)));
typedef float f32x2 __attribute__((ext_vector_type(2)));
__device__ __forceinline__ unsigned pk2(float lo, float hi) {
    const f32x2 v = {lo, hi};
    return __builtin_bit_cast(unsigned, __builtin_convertvector(v, bf16x2));
}
__device__ __forceinline__ float xl32_add(float v) { auto r = __builtin_amdgcn_permlane32_swap(__float_as_uint(v), __float_as_uint(v), false, false); return __uint_as_float(r[0]) + __uint_as_float(r[1]); }
__device__ __forceinline__ float xl16_add(float v) { auto r = __builtin_amdgcn_permlane16_swap(__float_as_uint(v), __float_as_uint(v), false, false); return __uint_as_float(r[0]) + __uint_as_float(r[1]); }
__device__ __forceinline__ float xl32_max(float v) { auto r = __builtin_amdgcn_permlane32_swap(__float_as_uint(v), __float_as_uint(v), false, false); return fmaxf(__uint_as_float(r[0]), __uint_as_float(r[1])); }
__device__ __forceinline__ float xl16_max(float v) { auto r = __builtin_amdgcn_permlane16_swap(__float_as_uint(v), __float_as_uint(v), false, false); return fmaxf(__uint_as_float(r[0]), __uint_as_float(r[1])); }
__device__ __forceinline__ float xl8_add(float v) { return v + __uint_as_float(__builtin_amdgcn_update_dpp(0, __float_as_uint(v), 0x128, 0xf, 0xf, false)); }
__device__ __forceinline__ float xl1_get(float v) { return __uint_as_float(__builtin_amdgcn_update_dpp(0, __float_as_uint(v), 0xB1, 0xf, 0xf, false)); }
#define SBAR() __builtin_amdgcn_sched_barrier(0)
__device__ __forceinline__ s16x4 tr_read(const LAS unsigned char* p) {
    return __builtin_amdgcn_ds_read_tr16_b64_v4i16((LAS s16x4*)p);
}
__device__ __forceinline__ bf16x8 cat8(s16x4 a, s16x4 b) {
    typedef short s16x8 __attribute__((ext_vector_type(8)));
    s16x8 r = __builtin_shufflevector(a, b, 0, 1, 2, 3, 4, 5, 6, 7);
    return __builtin_bit_cast(bf16x8, r);
}
__device__ __forceinline__ bf16x16 cat16(bf16x8 a, bf16x8 b) {
    return __builtin_shufflevector(a, b, 0, 1, 2, 3, 4, 5, 6, 7, 8, 9, 10, 11, 12, 13, 14, 15);
}
#define MFMA32(a, b, c) __builtin_amdgcn_mfma_f32_16x16x32_bf16((a), (b), (c), 0, 0, 0)
#define SMFMAC(a, b, c, idx) __builtin_amdgcn_smfmac_f32_16x16x64_bf16((a), (b), (c), (idx), 0, 0)

template <bool INIT>
__global__ void __launch_bounds__(256) k_ln(const u16* __restrict__ x, u16* __restrict__ y, unsigned char* __restrict__ ws) {
    const int row = blockIdx.x * 4 + (threadIdx.x >> 6), lane = threadIdx.x & 63;
    if (INIT) {
        const size_t u = (size_t)blockIdx.x * 256 + threadIdx.x;
        if (u * 16 < WS_SYNC_END) *(v4u*)(ws + u * 16) = (v4u){0xFFFFFFFFu, 0xFFFFFFFFu, 0xFFFFFFFFu, 0xFFFFFFFFu};
        else if (u * 16 < WS_FLAGS_END) *(v4u*)(ws + u * 16) = (v4u){0u, 0u, 0u, 0u};
    }
    const v4u raw = *(const v4u*)(x + (size_t)row * DM + lane * 8);
    float v[8];
#pragma unroll
    for (int i = 0; i < 4; ++i) { v[2 * i] = __uint_as_float(raw[i] << 16); v[2 * i + 1] = __uint_as_float(raw[i] & 0xffff0000u); }
    float s = 0.f;
#pragma unroll
    for (int i = 0; i < 8; ++i) s += v[i];
#pragma unroll
    for (int o = 32; o >= 1; o >>= 1) s += __shfl_xor(s, o);
    const float mean = s * (1.0f / DM);
    float q = 0.f;
#pragma unroll
    for (int i = 0; i < 8; ++i) { const float d = v[i] - mean; q += d * d; }
#pragma unroll
    for (int o = 32; o >= 1; o >>= 1) q += __shfl_xor(q, o);
    const float mb = rbf(mean), vb = rbf(q * (1.0f / DM));
    const float den = rbf(sqrtf(rbf(vb + 1e-5f)));
    v4u o;
#pragma unroll
    for (int i = 0; i < 4; ++i) o[i] = pk2(rbf(v[2 * i] - mb) / den, rbf(v[2 * i + 1] - mb) / den);
    *(v4u*)(y + (size_t)row * DM + lane * 8) = o;
}

constexpr int G_BK = 64, G_AST = 144;
__host__ __device__ constexpr int g_stage(int BM, int BN) { return BM * G_AST + G_BK * (BN * 2 + 32); }
__device__ __forceinline__ void st8_dev(u16* p, v2u v) {
    __hip_atomic_store((unsigned long long*)p, (unsigned long long)v[0] | ((unsigned long long)v[1] << 32), __ATOMIC_RELAXED, __HIP_MEMORY_SCOPE_AGENT);
}
template <int EPI, bool DEV, int BM, int BN>
__device__ __forceinline__ void gemm_tile(unsigned char* smem, const u16* __restrict__ A, int lda, const u16* __restrict__ B, const u16* __restrict__ B2, int ldb,
                                          u16* __restrict__ C, int ldc, const u16* __restrict__ res, int K, int m0, int n0) {
    constexpr int BST = BN * 2 + 32, ABYTES = BM * G_AST, STAGE = g_stage(BM, BN);
    constexpr int NA = BM / 32, NB = BN / 32, MI = BM / 32, NJ = BN / 32, BCH = BN / 8;
    const int t = threadIdx.x, w = t >> 6, l = t & 63, g = l >> 4, c16 = l & 15;
    const int wr = w >> 1, wc = w & 1;
    v4u ra[4][NA], rb[4][NB];
    auto gload = [&](int k0, v4u (&da)[NA], v4u (&db)[NB]) {
#pragma unroll
        for (int i = 0; i < NA; ++i) { const int u = i * 256 + t; da[i] = *(const v4u*)(A + (size_t)(m0 + (u >> 3)) * lda + k0 + (u & 7) * 8); }
#pragma unroll
        for (int i = 0; i < NB; ++i) {
            const int u = i * 256 + t, kr = u / BCH, ch = u % BCH;
            if (EPI == 2) {
                constexpr int Q = BCH / 4;
                const int wcc = ch / (2 * Q), part = (ch / Q) & 1, cc = (ch % Q) * 8;
                db[i] = *(const v4u*)((part ? B2 : B) + (size_t)(k0 + kr) * ldb + n0 + (BN / 4) * wcc + cc);
            } else db[i] = *(const v4u*)(B + (size_t)(k0 + kr) * ldb + n0 + ch * 8);
        }
    };
    auto swrite = [&](int buf, v4u (&da)[NA], v4u (&db)[NB]) {
        unsigned char* sa = smem + buf * STAGE; unsigned char* sb = sa + ABYTES;
#pragma unroll
        for (int i = 0; i < NA; ++i) { const int u = i * 256 + t; *(v4u*)(sa + (u >> 3) * G_AST + (u & 7) * 16) = da[i]; }
#pragma unroll
        for (int i = 0; i < NB; ++i) { const int u = i * 256 + t; *(v4u*)(sb + (u / BCH) * BST + (u % BCH) * 16) = db[i]; }
    };
    f32x4 acc[MI][NJ];
#pragma unroll
    for (int i = 0; i < MI; ++i)
#pragma unroll
        for (int j = 0; j < NJ; ++j) acc[i][j] = (f32x4){0.f, 0.f, 0.f, 0.f};
    bf16x8 af[2][MI], bfr[2][NJ];
    auto fread = [&](int buf) {
        const LAS unsigned char* sa = (const LAS unsigned char*)smem + buf * STAGE;
        const LAS unsigned char* sb = sa + ABYTES;
#pragma unroll
        for (int ks = 0; ks < 2; ++ks) {
#pragma unroll
            for (int i = 0; i < MI; ++i) af[ks][i] = *(const LAS bf16x8*)(sa + (wr * (BM / 2) + i * 16 + c16) * G_AST + (ks * 32 + g * 8) * 2);
#pragma unroll
            for (int j = 0; j < NJ; ++j) {
                const LAS unsigned char* p = sb + (ks * 32 + g * 8 + (c16 >> 2)) * BST + (wc * (BN / 2) + j * 16 + (c16 & 3) * 4) * 2;
                bfr[ks][j] = cat8(tr_read(p), tr_read(p + 4 * BST));
            }
        }
    };
    auto mma = [&](int ks) {
#pragma unroll
        for (int i = 0; i < MI; ++i)
#pragma unroll
            for (int j = 0; j < NJ; ++j) acc[i][j] = MFMA32(bfr[ks][j], af[ks][i], acc[i][j]);
    };
    const int nk = K / G_BK;
    const int klast = K - G_BK;
#pragma unroll
    for (int i = 0; i < 4; ++i) gload(i * G_BK, ra[i], rb[i]);
    __syncthreads();
    swrite(0, ra[0], rb[0]);
    gload(min(4 * G_BK, klast), ra[0], rb[0]);
    __syncthreads();
    for (int kt = 0; kt < nk; kt += 4) {
#pragma unroll
        for (int u = 0; u < 4; ++u) {
            fread(u & 1);
            mma(0);
            swrite((u + 1) & 1, ra[(u + 1) & 3], rb[(u + 1) & 3]);
            gload(min((kt + u + 5) * G_BK, klast), ra[(u + 1) & 3], rb[(u + 1) & 3]);
            mma(1);
            __syncthreads();
        }
    }
    if (EPI == 4) {
        const auto rC = __builtin_amdgcn_make_buffer_rsrc((void*)C, 0, (unsigned)S_LEN * DM * 4, 0x00020000);
#pragma unroll
        for (int i = 0; i < MI; ++i)
#pragma unroll
            for (int j = 0; j < NJ; ++j)
                __builtin_amdgcn_raw_buffer_store_b128(__builtin_bit_cast(v4u, acc[i][j]), rC, (unsigned)((m0 + wr * (BM / 2) + i * 16 + c16) * ldc + n0 + wc * (BN / 2) + j * 16 + 4 * g) * 4u, 0, DEV ? 16 : 0);
        return;
    }
    constexpr int ON = EPI == 2 ? BN / 2 : BN, OST = ON * 2 + 16;
#pragma unroll
    for (int i = 0; i < MI; ++i) {
        const int m = m0 + wr * (BM / 2) + i * 16 + c16;
        if (EPI == 2) {
#pragma unroll
            for (int j = 0; j < NJ / 2; ++j) {
                float o[4];
#pragma unroll
                for (int r = 0; r < 4; ++r) {
                    const float gt = rbf(acc[i][j][r]), up = rbf(acc[i][j + NJ / 2][r]);
                    const float inner = 0.7978845608028654f * (gt + 0.044715f * gt * gt * gt);
                    const float th = 1.0f - 2.0f * __builtin_amdgcn_rcpf(__expf(2.0f * inner) + 1.0f);
                    const float ge = rbf(0.5f * gt * (1.0f + th));
                    o[r] = ge * up;
                }
                v2u st = {pk2(o[0], o[1]), pk2(o[2], o[3])};
                u16* dst = C + (size_t)m * ldc + n0 + wc * (BN / 4) + j * 16 + 4 * g;
                if (DEV) *(v2u*)(smem + (m - m0) * OST + (wc * (BN / 4) + j * 16 + 4 * g) * 2) = st; else *(v2u*)dst = st;
            }
        } else {
#pragma unroll
            for (int j = 0; j < NJ; ++j) {
                const int n = n0 + wc * (BN / 2) + j * 16 + 4 * g;
                float o[4];
#pragma unroll
                for (int r = 0; r < 4; ++r) o[r] = acc[i][j][r];
                if (EPI == 1) {
                    const v2u rr = *(const v2u*)(res + (size_t)m * ldc + n);
                    o[0] = __uint_as_float(rr[0] << 16) + rbf(o[0]); o[1] = __uint_as_float(rr[0] & 0xffff0000u) + rbf(o[1]);
                    o[2] = __uint_as_float(rr[1] << 16) + rbf(o[2]); o[3] = __uint_as_float(rr[1] & 0xffff0000u) + rbf(o[3]);
                }
                v2u st = {pk2(o[0], o[1]), pk2(o[2], o[3])};
                if (DEV) *(v2u*)(smem + (m - m0) * OST + (n - n0) * 2) = st; else *(v2u*)(C + (size_t)m * ldc + n) = st;
            }
        }
    }
    if (DEV) {
        __syncthreads();
        constexpr int CH = ON / 8;
        const auto rC = __builtin_amdgcn_make_buffer_rsrc((void*)C, 0, 0x7fffffffu, 0x00020000);
#pragma unroll
        for (int i = 0; i < BM * CH / 256; ++i) {
            const int u = i * 256 + t, row = u / CH, ch = u % CH;
            __builtin_amdgcn_raw_buffer_store_b128(*(const v4u*)(smem + row * OST + ch * 16), rC, (unsigned)((m0 + row) * ldc + n0 + ch * 8) * 2u, 0, 16);
        }
    }
}
template <int EPI, int BM, int BN>
__global__ void __launch_bounds__(256) k_gemm(const u16* __restrict__ A, int lda, const u16* __restrict__ B, const u16* __restrict__ B2, int ldb,
                                              u16* __restrict__ C, int ldc, const u16* __restrict__ res, int K, int row0) {
    extern __shared__ __attribute__((aligned(16))) unsigned char smem[];
    gemm_tile<EPI, false, BM, BN>(smem, A, lda, B, B2, ldb, C, ldc, res, K, row0 + blockIdx.y * BM, blockIdx.x * (EPI == 2 ? BN / 2 : BN));
}

template <int BM, int BN>
__global__ void __launch_bounds__(256) k_gemm_splitk(const u16* __restrict__ A, int lda, const u16* __restrict__ B, int ldb, float* __restrict__ slab, int Kpart, int row0) {
    extern __shared__ __attribute__((aligned(16))) unsigned char smem[];
    const int z = blockIdx.z;
    gemm_tile<4, false, BM, BN>(smem, A + (size_t)z * Kpart, lda, B + (size_t)z * Kpart * ldb, nullptr, ldb, (u16*)(slab + (size_t)z * S_LEN * DM), DM, nullptr, Kpart, row0 + blockIdx.y * BM, blockIdx.x * BN);
}
template <int NS, bool LN>
__global__ void __launch_bounds__(256) k_reduce(const float* __restrict__ slab, const u16* __restrict__ res, u16* __restrict__ hout, u16* __restrict__ nout, int row0) {
    const int row = row0 + blockIdx.x * 4 + (threadIdx.x >> 6), l = threadIdx.x & 63;
    f32x4 a0 = (f32x4){0.f, 0.f, 0.f, 0.f}, a1 = a0;
#pragma unroll
    for (int ks = 0; ks < NS; ++ks) {
        const float* src = slab + ((size_t)ks * S_LEN + row) * DM + l * 8;
        a0 += *(const f32x4*)src; a1 += *(const f32x4*)(src + 4);
    }
    const v4u xr = *(const v4u*)(res + (size_t)row * DM + l * 8);
    float v[8];
#pragma unroll
    for (int i = 0; i < 4; ++i) {
        const float lo = i < 2 ? a0[2 * i] : a1[2 * i - 4], hi = i < 2 ? a0[2 * i + 1] : a1[2 * i - 3];
        v[2 * i] = rbf(__uint_as_float(xr[i] << 16) + rbf(lo)); v[2 * i + 1] = rbf(__uint_as_float(xr[i] & 0xffff0000u) + rbf(hi));
    }
    *(v4u*)(hout + (size_t)row * DM + l * 8) = (v4u){pk2(v[0], v[1]), pk2(v[2], v[3]), pk2(v[4], v[5]), pk2(v[6], v[7])};
    if (LN) {
        float sm = 0.f;
#pragma unroll
        for (int i = 0; i < 8; ++i) sm += v[i];
#pragma unroll
        for (int o = 32; o >= 1; o >>= 1) sm += __shfl_xor(sm, o);
        const float mean = sm * (1.0f / DM);
        float q = 0.f;
#pragma unroll
        for (int i = 0; i < 8; ++i) { const float d = v[i] - mean; q += d * d; }
#pragma unroll
        for (int o = 32; o >= 1; o >>= 1) q += __shfl_xor(q, o);
        const float mb = rbf(mean), den = rbf(sqrtf(rbf(rbf(q * (1.0f / DM)) + 1e-5f)));
        v4u o;
#pragma unroll
        for (int i = 0; i < 4; ++i) o[i] = pk2(rbf(v[2 * i] - mb) / den, rbf(v[2 * i + 1] - mb) / den);
        *(v4u*)(nout + (size_t)row * DM + l * 8) = o;
    }
}

constexpr unsigned SENT = 0xFFFFFFFFu;
constexpr int C_WV = 0;
constexpr int C_MEM = 131072;
constexpr int RST = 544;
constexpr int C_QS = C_MEM + 8192;
constexpr int C_KS = C_QS + 256;
constexpr int C_VS = C_QS + 8 * RST;
constexpr int C_KX = C_VS + 2048;
constexpr int C_VX = C_KX + 256;
constexpr int C_LDS = C_KX + 32 * RST;
constexpr unsigned SPIN_CAP = 1u << 22;
#define DLY1 14
#define DLY2 12

__device__ __forceinline__ __amdgpu_buffer_rsrc_t mk_rsrc(const void* p, unsigned bytes) {
    return __builtin_amdgcn_make_buffer_rsrc((void*)p, 0, bytes, 0x00020000);
}
__device__ __forceinline__ v4u ld16_sc1(__amdgpu_buffer_rsrc_t r, unsigned off) { return __builtin_amdgcn_raw_buffer_load_b128(r, off, 0, 16); }
__device__ __forceinline__ void st16_sc1(v4u v, __amdgpu_buffer_rsrc_t r, unsigned off) { __builtin_amdgcn_raw_buffer_store_b128(v, r, off, 0, 16); }
__device__ __forceinline__ void st4_sc1(unsigned v, __amdgpu_buffer_rsrc_t r, unsigned off) { __builtin_amdgcn_raw_buffer_store_b32(v, r, off, 0, 16); }
__device__ __forceinline__ void dly(int n) { for (int i = 0; i < n; ++i) __builtin_amdgcn_s_sleep(1); }

__device__ __forceinline__ f32x4 comb2(f32x4 a, f32x4 b) {
    f32x4 o;
#pragma unroll
    for (int r = 0; r < 4; ++r) {
        auto sw = __builtin_amdgcn_permlane32_swap(__float_as_uint(a[r]), __float_as_uint(b[r]), false, false);
        o[r] = __uint_as_float(sw[0]) + __uint_as_float(sw[1]);
    }
    return o;
}

__device__ __forceinline__ unsigned ld_flag(const unsigned* p) { return __hip_atomic_load(p, __ATOMIC_RELAXED, __HIP_MEMORY_SCOPE_AGENT); }
__device__ __forceinline__ void wait_flag_ge(const unsigned* p, unsigned want, int sleep_n) {
    unsigned spins = 0;
    while ((unsigned)__builtin_amdgcn_readfirstlane(ld_flag(p)) < want) { dly(sleep_n); if (++spins > SPIN_CAP) break; }
}
__device__ __forceinline__ void wait_flag_kv(const unsigned* p) {
    unsigned spins = 0;
    while (((unsigned)__builtin_amdgcn_readfirstlane(ld_flag(p)) & 3u) != 2u) { dly(2); if (++spins > SPIN_CAP) break; }
}
__device__ __forceinline__ void chain_body(unsigned char* smem, const u16* __restrict__ Wq, const u16* __restrict__ Wk, const u16* __restrict__ Wv,
                                           const u16* __restrict__ Wo, const u16* __restrict__ mem0,
                                           const u16* Kx, const u16* Vx, u16* kmem, u16* vmem, unsigned char* ws_sync) {
    LAS unsigned char* L = (LAS unsigned char*)smem;
    unsigned* flags = (unsigned*)(ws_sync + WS_FLAGS);
    const auto rK = mk_rsrc(Kx, (unsigned)S_LEN * HD * 2);
    const auto rV = mk_rsrc(Vx, (unsigned)S_LEN * HD * 2);
    const auto rKM = mk_rsrc(kmem, (unsigned)NSEG * NH * MM * DH * 2);
    const auto rVM = mk_rsrc(vmem, (unsigned)NSEG * NH * MM * DH * 2);
    const int h = blockIdx.x, t = threadIdx.x, w = __builtin_amdgcn_readfirstlane(t >> 6), l = t & 63, g = l >> 4, c16 = l & 15;
    const auto rRS = mk_rsrc(ws_sync + WS_RS, 2u * 32 * 32 * 512);
    const auto rAG = mk_rsrc(ws_sync + WS_AG, 2u * 8192);
    const int sidx = (c16 < 8) ? 0x4444 : 0xEEEE;

    bf16x16 Bq[2][8], Bk[2][8], Bo[8][2];
    {
        constexpr int ST = C_MEM;
        v4u P[4][4];
        auto gl = [&](int r, v4u (&d)[4]) {
#pragma unroll
            for (int i = 0; i < 4; ++i) {
                const int u = i * 256 + t, k = u >> 4, ch = u & 15;
                if (r < 24) {
                    const u16* W = r < 8 ? Wq : (r < 16 ? Wk : Wv);
                    d[i] = *(const v4u*)(W + (size_t)(64 * (r & 7) + k) * HD + h * DH + ch * 8);
                } else {
                    const int kk = (r - 24) >> 2, cb = (r - 24) & 3;
                    const int gg = k >> 4, wi = k & 15, ii = 2 * (wi >> 2) + (wi & 1), cc = (wi >> 1) & 1;
                    const int e = 64 * kk + 32 * cc + 8 * gg + ii;
                    d[i] = *(const v4u*)(Wo + ((size_t)h * DH + e) * DM + cb * 128 + ch * 8);
                }
            }
        };
#pragma unroll
        for (int r = 0; r < 4; ++r) gl(r, P[r]);
#pragma unroll
        for (int r = 0; r < 32; ++r) {
            __syncthreads();
#pragma unroll
            for (int i = 0; i < 4; ++i) { const int u = i * 256 + t; *(LAS v4u*)(L + ST + (u >> 4) * 288 + (u & 15) * 16) = P[r & 3][i]; }
            if (r + 4 < 32) gl(r + 4, P[r & 3]);
            __syncthreads();
            if (r < 24) {
                const int mat = r >> 3, ks = r & 7;
#pragma unroll
                for (int tt = 0; tt < 2; ++tt) {
                    const LAS unsigned char* p = L + ST + (8 * g + (c16 >> 2)) * 288 + (32 * w + 16 * tt + (c16 & 3) * 4) * 2;
                    const bf16x8 lo = cat8(tr_read(p), tr_read(p + 4 * 288));
                    const bf16x8 hi = cat8(tr_read(p + 32 * 288), tr_read(p + 36 * 288));
                    if (mat == 0) Bq[tt][ks] = cat16(lo, hi);
                    else if (mat == 1) Bk[tt][ks] = cat16(lo, hi);
                    else {
                        *(LAS bf16x8*)(L + C_WV + (((2 * w + tt) * 8 + ks) * 2 + 0) * 1024 + l * 16) = lo;
                        *(LAS bf16x8*)(L + C_WV + (((2 * w + tt) * 8 + ks) * 2 + 1) * 1024 + l * 16) = hi;
                    }
                }
            } else {
                const int kk = (r - 24) >> 2, cb = (r - 24) & 3;
                if (w == cb) {
#pragma unroll
                    for (int tt = 0; tt < 8; ++tt) {
                        const LAS unsigned char* p = L + ST + (8 * g + (c16 >> 2)) * 288 + (16 * tt + (c16 & 3) * 4) * 2;
                        const bf16x8 lo = cat8(tr_read(p), tr_read(p + 4 * 288));
                        const bf16x8 hi = cat8(tr_read(p + 32 * 288), tr_read(p + 36 * 288));
                        Bo[tt][kk] = cat16(lo, hi);
                    }
                }
            }
        }
        __syncthreads();
    }
    {
#pragma unroll
        for (int i = 0; i < 2; ++i) {
            const int u = i * 256 + t, ks = u >> 6, ll = u & 63, gg = ll >> 4, cc = (ll >> 3) & 1, m = ll & 7;
            const u16* src = mem0 + m * DM + 64 * ks + 16 * gg + 2 * cc;
            v4u v;
#pragma unroll
            for (int q = 0; q < 4; ++q) v[q] = *(const unsigned*)(src + 4 * q);
            *(LAS v4u*)(L + C_MEM + u * 16) = v;
        }
        wait_flag_kv(flags + FL_QKV + h * 16);
#pragma unroll
        for (int i = 0; i < 2; ++i) {
            const int u = i * 256 + t, r = u >> 4, ch = u & 15;
            *(LAS v4u*)(L + C_KX + r * RST + ch * 16) = ld16_sc1(rK, (unsigned)(r * HD + h * DH + ch * 8) * 2u);
            *(LAS v4u*)(L + C_VX + r * RST + ch * 16) = ld16_sc1(rV, (unsigned)(r * HD + h * DH + ch * 8) * 2u);
        }
    }
    __syncthreads();

#define PIN(x) asm volatile("" : "+v"(x))
    for (int s = 0; s < NSEG; ++s) {
        int t = threadIdx.x; PIN(t);
        const int l = t & 63, g = l >> 4, c16 = l & 15;
        unsigned oWV = (unsigned)(size_t)(L + C_WV + (2 * w * 16) * 1024 + l * 16);
        unsigned oMEM = (unsigned)(size_t)(L + C_MEM + l * 16);
        unsigned oKX = (unsigned)(size_t)(L + C_KX + c16 * RST + 16 * g);
        unsigned oQK = (unsigned)(size_t)(L + C_QS + (c16 & 7) * RST + 16 * g);
        unsigned oQW = (unsigned)(size_t)(L + C_QS + 4 * (g & 1) * RST + (32 * w + 16 * (l >> 5) + c16) * 2);
        unsigned oVW = (unsigned)(size_t)(L + C_VS + (32 * w + 16 * (l >> 5) + c16) * 16 + (g & 1) * 8);
        PIN(oWV); PIN(oMEM); PIN(oKX); PIN(oQK); PIN(oQW); PIN(oVW);
#define LP(o) ((LAS unsigned char*)(size_t)(o))
        f32x4 aq[2], ak[2], av[2];
#pragma unroll
        for (int i = 0; i < 2; ++i) { aq[i] = (f32x4){0.f, 0.f, 0.f, 0.f}; ak[i] = aq[i]; av[i] = aq[i]; PIN(aq[i]); PIN(ak[i]); PIN(av[i]); }
        bf16x8 A[8], Vb[2][4];
#pragma unroll
        for (int i = 0; i < 4; ++i) Vb[0][i] = *(const LAS bf16x8*)(LP(oWV) + ((i >> 1) * 16 + (i & 1)) * 1024);
        if (s > 0) {
            const unsigned slot = (s - 1) & 1;
            dly(DLY2);
            v4u x[2];
            unsigned spins = 0;
            for (;;) {
                asm volatile("" ::: "memory");
                bool ok = true;
#pragma unroll
                for (int i = 0; i < 2; ++i) x[i] = ld16_sc1(rAG, slot * 8192u + (i * 256 + t) * 16u);
#pragma unroll
                for (int i = 0; i < 2; ++i) ok &= (x[i][0] != SENT) & (x[i][1] != SENT) & (x[i][2] != SENT) & (x[i][3] != SENT);
                if (__all(ok)) break;
                if (++spins > SPIN_CAP) break;
            }
#pragma unroll
            for (int i = 0; i < 2; ++i) *(LAS v4u*)(LP(oMEM) + (i * 256 + w * 64) * 16) = x[i];
            __syncthreads();
            if (t == 0) __hip_atomic_fetch_add(flags + FL_PROG, 1u, __ATOMIC_RELAXED, __HIP_MEMORY_SCOPE_AGENT);
        }
#pragma unroll
        for (int ks = 0; ks < 2; ++ks) A[ks] = *(const LAS bf16x8*)(LP(oMEM) + ks * 1024);
        SBAR();
#pragma unroll
        for (int ks = 0; ks < 8; ++ks) {
            if (ks < 6) A[ks + 2] = *(const LAS bf16x8*)(LP(oMEM) + (ks + 2) * 1024);
            if (ks + 1 < 8) {
#pragma unroll
                for (int i = 0; i < 4; ++i) Vb[(ks + 1) & 1][i] = *(const LAS bf16x8*)(LP(oWV) + ((i >> 1) * 16 + (ks + 1) * 2 + (i & 1)) * 1024);
            }
            SBAR();
            aq[0] = SMFMAC(A[ks], Bq[0][ks], aq[0], sidx);
            aq[1] = SMFMAC(A[ks], Bq[1][ks], aq[1], sidx);
            ak[0] = SMFMAC(A[ks], Bk[0][ks], ak[0], sidx);
            ak[1] = SMFMAC(A[ks], Bk[1][ks], ak[1], sidx);
            av[0] = SMFMAC(A[ks], cat16(Vb[ks & 1][0], Vb[ks & 1][1]), av[0], sidx);
            av[1] = SMFMAC(A[ks], cat16(Vb[ks & 1][2], Vb[ks & 1][3]), av[1], sidx);
            SBAR();
        }
        bf16x8 kx[2][4];
#pragma unroll
        for (int kq = 0; kq < 4; ++kq) {
            kx[0][kq] = *(const LAS bf16x8*)(LP(oKX) + 64 * kq);
            kx[1][kq] = *(const LAS bf16x8*)(LP(oKX) + 16 * RST + 64 * kq);
        }
        {
            const f32x4 q = comb2(aq[0], aq[1]), k = comb2(ak[0], ak[1]), v = comb2(av[0], av[1]);
            const unsigned q01 = pk2(q[0], q[1]), q23 = pk2(q[2], q[3]), k01 = pk2(k[0], k[1]), k23 = pk2(k[2], k[3]);
            LAS unsigned char* pq = LP(oQW);
            LAS unsigned char* pk = LP(oQW) + (C_KS - C_QS);
            *(LAS u16*)(pq) = (u16)q01; *(LAS u16*)(pq + RST) = (u16)(q01 >> 16); *(LAS u16*)(pq + 2 * RST) = (u16)q23; *(LAS u16*)(pq + 3 * RST) = (u16)(q23 >> 16);
            *(LAS u16*)(pk) = (u16)k01; *(LAS u16*)(pk + RST) = (u16)(k01 >> 16); *(LAS u16*)(pk + 2 * RST) = (u16)k23; *(LAS u16*)(pk + 3 * RST) = (u16)(k23 >> 16);
            v2u vv = {pk2(v[0], v[1]), pk2(v[2], v[3])};
            *(LAS v2u*)(LP(oVW)) = vv;
        }
        unsigned oVX = (unsigned)(size_t)(L + C_VX + (4 * g + (c16 >> 2)) * RST + (c16 & 3) * 8 + 64 * w);
        unsigned oVM = (unsigned)(size_t)(L + C_VS + c16 * 16 + (g & 1) * 8 + 512 * w);
        unsigned oOW = (unsigned)(size_t)(L + C_MEM + (c16 & 7) * RST + (32 * w + 4 * g) * 2);
        unsigned oOR = (unsigned)(size_t)(L + C_MEM + (c16 & 7) * RST + (32 * (c16 >> 3) + 8 * g) * 2);
        PIN(oVX); PIN(oVM); PIN(oOW); PIN(oOR);
        f32x4 oo[8];
        __syncthreads();
        if (s == NSEG - 1) break;
        bf16x8 aO[2];
        {
            bf16x8 qb[4], km_[4];
#pragma unroll
            for (int kq = 0; kq < 4; ++kq) {
                qb[kq] = *(const LAS bf16x8*)(LP(oQK) + 64 * kq);
                km_[kq] = *(const LAS bf16x8*)(LP(oQK) + (C_KS - C_QS) + 64 * kq);
            }
            bf16x8 va[2]; v2u vm[2];
#pragma unroll
            for (int T = 0; T < 2; ++T) {
                va[T] = cat8(tr_read(LP(oVX) + 32 * T), tr_read(LP(oVX) + 32 * T + 16 * RST));
                vm[T] = *(const LAS v2u*)(LP(oVM) + 256 * T);
            }
            SBAR();
            f32x4 sc[3];
#pragma unroll
            for (int T = 0; T < 3; ++T) sc[T] = (f32x4){0.f, 0.f, 0.f, 0.f};
#pragma unroll
            for (int kq = 0; kq < 4; ++kq) {
                sc[0] = MFMA32(kx[0][kq], qb[kq], sc[0]); sc[1] = MFMA32(kx[1][kq], qb[kq], sc[1]); sc[2] = MFMA32(km_[kq], qb[kq], sc[2]);
            }
            SBAR();
            const float cs = 0.08838834764831845f * 1.4426950408889634f;
            if (g >= 2) sc[2] = (f32x4){-3.0e38f, -3.0e38f, -3.0e38f, -3.0e38f};
            float mx = fmaxf(fmaxf(fmaxf(sc[0][0], sc[0][1]), fmaxf(sc[0][2], sc[0][3])), fmaxf(fmaxf(sc[1][0], sc[1][1]), fmaxf(sc[1][2], sc[1][3])));
            mx = fmaxf(mx, fmaxf(fmaxf(sc[2][0], sc[2][1]), fmaxf(sc[2][2], sc[2][3])));
            mx = xl32_max(xl16_max(mx));
            const float nm = -mx * cs;
            float p[12];
#pragma unroll
            for (int T = 0; T < 3; ++T)
#pragma unroll
                for (int r = 0; r < 4; ++r) p[T * 4 + r] = __builtin_amdgcn_exp2f(__builtin_fmaf(sc[T][r], cs, nm));
            float sum = ((p[0] + p[1]) + (p[2] + p[3])) + ((p[4] + p[5]) + (p[6] + p[7])) + ((p[8] + p[9]) + (p[10] + p[11]));
            sum = xl32_add(xl16_add(sum));
            const float inv = __builtin_amdgcn_rcpf(sum);
            v4u px = {pk2(p[0] * inv, p[1] * inv), pk2(p[2] * inv, p[3] * inv), pk2(p[4] * inv, p[5] * inv), pk2(p[6] * inv, p[7] * inv)};
            const bf16x8 Px = __builtin_bit_cast(bf16x8, px);
            const bf16x8 Pm = __builtin_bit_cast(bf16x8, (v4u){pk2(p[8] * inv, p[9] * inv), pk2(p[10] * inv, p[11] * inv), 0u, 0u});
            f32x4 o[2];
#pragma unroll
            for (int T = 0; T < 2; ++T) o[T] = MFMA32(va[T], Px, ((f32x4){0.f, 0.f, 0.f, 0.f}));
#pragma unroll
            for (int T = 0; T < 2; ++T) o[T] = MFMA32(__builtin_bit_cast(bf16x8, (v4u){vm[T][0], vm[T][1], 0u, 0u}), Pm, o[T]);
            if (c16 < 8) {
#pragma unroll
                for (int T = 0; T < 2; ++T) *(LAS v2u*)(LP(oOW) + 32 * T) = (v2u){pk2(o[T][0], o[T][1]), pk2(o[T][2], o[T][3])};
            }
#pragma unroll
            for (int tt = 0; tt < 8; ++tt) { oo[tt] = (f32x4){0.f, 0.f, 0.f, 0.f}; PIN(oo[tt]); }
            __syncthreads();
            aO[0] = *(const LAS bf16x8*)(LP(oOR));
            aO[1] = *(const LAS bf16x8*)(LP(oOR) + 128);
        }
        const unsigned slot = s & 1, par = (s >> 1) & 1;
        {
#pragma unroll
            for (int tt = 0; tt < 8; ++tt) oo[tt] = SMFMAC(aO[0], Bo[tt][0], oo[tt], sidx);
#pragma unroll
            for (int tt = 0; tt < 8; ++tt) oo[tt] = SMFMAC(aO[1], Bo[tt][1], oo[tt], sidx);
#pragma unroll
            for (int tp = 0; tp < 4; ++tp) {
                const f32x4 o = comb2(oo[2 * tp], oo[2 * tp + 1]);
                v4u st;
#pragma unroll
                for (int r = 0; r < 4; ++r) st[r] = (__float_as_uint(o[r]) & ~1u) | par;
                const unsigned cons = 8 * w + 2 * tp + (l >> 5);
                st16_sc1(st, rRS, slot * 524288u + cons * 16384u + h * 512u + (l & 31) * 16u);
            }
        }
        {
            const int u = t & 127;
            const v4u pub = *(const LAS v4u*)(L + (t < 128 ? C_KS + (u >> 4) * RST + (u & 15) * 16 : C_VS + u * 16));
            const unsigned off = (unsigned)((s * NH + h) * (MM * DH) + u * 8) * 2u;
            if (t < 128) st16_sc1(pub, rKM, off); else st16_sc1(pub, rVM, off);
        }
        if (((s + 1) & 3) == 0) wait_flag_kv(flags + FL_QKV + (((s + 1) >> 2) * 32 + h) * 16);
        v4u pk_[2], pv_[2];
#pragma unroll
        for (int i = 0; i < 2; ++i) {
            const int u = i * 256 + t, r = u >> 4, ch = u & 15;
            pk_[i] = ld16_sc1(rK, (unsigned)(((s + 1) * SEG + r) * HD + h * DH + ch * 8) * 2u);
            pv_[i] = ld16_sc1(rV, (unsigned)(((s + 1) * SEG + r) * HD + h * DH + ch * 8) * 2u);
        }
        const int lr = (l >> 3) & 3;
        unsigned offAG;
        {
            const int m = 4 * (w >> 1) + lr, cp = 4 * (w & 1) + ((l & 7) >> 1);
            offAG = slot * 8192u + (h >> 2) * 1024u + ((h & 3) * 16 + (cp & 1) * 8 + m) * 16u + (cp >> 1) * 4u;
            PIN(offAG);
        }
        dly(DLY1);
        {
            v4u x[4];
            const unsigned base = slot * 524288u + h * 16384u + (l >> 3) * 512u + (8 * w + (l & 7)) * 16u;
            unsigned spins = 0;
            for (;;) {
                asm volatile("" ::: "memory");
                unsigned bad = 0;
#pragma unroll
                for (int i = 0; i < 4; ++i) x[i] = ld16_sc1(rRS, base + i * 4096u);
#pragma unroll
                for (int i = 0; i < 4; ++i) bad |= (x[i][0] ^ par) | (x[i][1] ^ par) | (x[i][2] ^ par) | (x[i][3] ^ par);
                if (__all((bad & 1u) == 0)) break;
                if (++spins > SPIN_CAP) break;
            }
            float sm[4];
#pragma unroll
            for (int r = 0; r < 4; ++r) {
                float v = (__uint_as_float(x[0][r]) + __uint_as_float(x[1][r])) + (__uint_as_float(x[2][r]) + __uint_as_float(x[3][r]));
                sm[r] = xl32_add(xl16_add(xl8_add(v)));
            }
            const float own = lr == 0 ? sm[0] : (lr == 1 ? sm[1] : (lr == 2 ? sm[2] : sm[3]));
            const float nb = xl1_get(own);
            unsigned pkd = pk2(own, nb);
            if (pkd == SENT) pkd = 0x7FC07FC0u;
            if (l < 32 && !(l & 1)) {
                st4_sc1(pkd, rAG, offAG);
                st4_sc1(SENT, rAG, offAG ^ 8192u);
            }
        }
#pragma unroll
        for (int i = 0; i < 2; ++i) {
            const int u = i * 256 + t, r = u >> 4, ch = u & 15;
            *(LAS v4u*)(L + C_KX + r * RST + ch * 16) = pk_[i];
            *(LAS v4u*)(L + C_VX + r * RST + ch * 16) = pv_[i];
        }
    }
    {
        const int u = t & 127;
        const v4u pub = *(const LAS v4u*)(L + (t < 128 ? C_KS + (u >> 4) * RST + (u & 15) * 16 : C_VS + u * 16));
        const unsigned off = (unsigned)(((NSEG - 1) * NH + h) * (MM * DH) + u * 8) * 2u;
        if (t < 128) st16_sc1(pub, rKM, off); else st16_sc1(pub, rVM, off);
        asm volatile("s_waitcnt vmcnt(0)" ::: "memory");
        __syncthreads();
        if (t == 0) __hip_atomic_fetch_add(flags + FL_PROG, 1u, __ATOMIC_RELAXED, __HIP_MEMORY_SCOPE_AGENT);
    }
}

__device__ __forceinline__ void xattn_task(unsigned char* smem, const u16* Q, const u16* Kx, const u16* Vx,
                                           const u16* kmem, const u16* vmem, u16* __restrict__ O, int s, int h, const unsigned* prog) {
    const int t = threadIdx.x, w = t >> 6, l = t & 63, g = l >> 4, c16 = l & 15;
    LAS unsigned char* LV = (LAS unsigned char*)smem + w * 32 * 288;
    LAS unsigned char* LO = (LAS unsigned char*)smem + 4 * 32 * 288 + w * 32 * 272;
    const size_t rowb = (size_t)s * SEG;
#pragma unroll
    for (int i = 0; i < 8; ++i) {
        const int u = i * 64 + l, r = u >> 4, ch = u & 15;
        *(LAS v4u*)(LV + r * 288 + ch * 16) = *(const v4u*)(Vx + (rowb + r) * HD + h * DH + ch * 8);
    }
    f32x4 sc[2][3];
#pragma unroll
    for (int qt = 0; qt < 2; ++qt)
#pragma unroll
        for (int T = 0; T < 3; ++T) sc[qt][T] = (f32x4){0.f, 0.f, 0.f, 0.f};
    bf16x8 q0[4], q1[4];
#pragma unroll
    for (int kq = 0; kq < 4; ++kq) {
        const int eo = 32 * kq + 8 * g;
        q0[kq] = *(const bf16x8*)(Q + (rowb + c16) * HD + h * DH + eo);
        q1[kq] = *(const bf16x8*)(Q + (rowb + 16 + c16) * HD + h * DH + eo);
        const bf16x8 k0 = *(const bf16x8*)(Kx + (rowb + c16) * HD + h * DH + eo);
        const bf16x8 k1 = *(const bf16x8*)(Kx + (rowb + 16 + c16) * HD + h * DH + eo);
        sc[0][0] = MFMA32(k0, q0[kq], sc[0][0]); sc[0][1] = MFMA32(k1, q0[kq], sc[0][1]);
        sc[1][0] = MFMA32(k0, q1[kq], sc[1][0]); sc[1][1] = MFMA32(k1, q1[kq], sc[1][1]);
    }
    if (w == 0) {
        unsigned spins = 0;
        const int nap = s >= NSEG - 2 ? 4 : 40;
        while ((unsigned)__builtin_amdgcn_readfirstlane(ld_flag(prog)) < (unsigned)(s + 1) * NH) { dly(nap); if (++spins > SPIN_CAP) break; }
    }
    __syncthreads();
    const auto rKM = mk_rsrc(kmem, (unsigned)NSEG * NH * MM * DH * 2);
    const auto rVM = mk_rsrc(vmem, (unsigned)NSEG * NH * MM * DH * 2);
    v2u vmf[8];
#pragma unroll
    for (int T = 0; T < 8; ++T) vmf[T] = __builtin_amdgcn_raw_buffer_load_b64(rVM, (unsigned)((s * NH + h) * (MM * DH) + (16 * T + c16) * 8 + (g & 1) * 4) * 2u, 0, 16);
#pragma unroll
    for (int kq = 0; kq < 4; ++kq) {
        const bf16x8 k2 = __builtin_bit_cast(bf16x8, ld16_sc1(rKM, (unsigned)(((s * NH + h) * MM + (c16 & 7)) * DH + 32 * kq + 8 * g) * 2u));
        sc[0][2] = MFMA32(k2, q0[kq], sc[0][2]); sc[1][2] = MFMA32(k2, q1[kq], sc[1][2]);
    }
    bf16x8 Px[2], Pm[2];
    const float scale = 0.08838834764831845f;
#pragma unroll
    for (int qt = 0; qt < 2; ++qt) {
        const int qi = 16 * qt + c16;
        float p[12]; float mx = -3.0e38f;
#pragma unroll
        for (int T = 0; T < 3; ++T)
#pragma unroll
            for (int r = 0; r < 4; ++r) {
                float v = rbf(rbf(sc[qt][T][r]) * scale);
                const bool valid = (T == 2) ? (g < 2) : (16 * T + 4 * g + r <= qi);
                if (!valid) v = -3.0e38f;
                p[T * 4 + r] = v; mx = fmaxf(mx, v);
            }
        mx = fmaxf(mx, __shfl_xor(mx, 16)); mx = fmaxf(mx, __shfl_xor(mx, 32));
        float sum = 0.f;
#pragma unroll
        for (int i = 0; i < 12; ++i) { p[i] = (p[i] < -1.0e38f) ? 0.f : __expf(p[i] - mx); sum += p[i]; }
        sum += __shfl_xor(sum, 16); sum += __shfl_xor(sum, 32);
        const float inv = 1.0f / sum;
        v4u px = {pk2(p[0] * inv, p[1] * inv), pk2(p[2] * inv, p[3] * inv), pk2(p[4] * inv, p[5] * inv), pk2(p[6] * inv, p[7] * inv)};
        v2u pm = {pk2(p[8] * inv, p[9] * inv), pk2(p[10] * inv, p[11] * inv)};
        Px[qt] = __builtin_bit_cast(bf16x8, px); Pm[qt] = __builtin_bit_cast(bf16x8, (v4u){pm[0], pm[1], 0u, 0u});
    }
#pragma unroll
    for (int T = 0; T < 8; ++T) {
        const LAS unsigned char* pv = LV + (4 * g + (c16 >> 2)) * 288 + (16 * T + (c16 & 3) * 4) * 2;
        const bf16x8 va = cat8(tr_read(pv), tr_read(pv + 16 * 288));
        v2u vm = vmf[T];
        if (g >= 2) vm = (v2u){0u, 0u};
#pragma unroll
        for (int qt = 0; qt < 2; ++qt) {
            f32x4 o = (f32x4){0.f, 0.f, 0.f, 0.f};
            o = MFMA32(va, Px[qt], o);
            o = MFMA32(__builtin_bit_cast(bf16x8, (v4u){vm[0], vm[1], 0u, 0u}), Pm[qt], o);
            v2u st = {pk2(o[0], o[1]), pk2(o[2], o[3])};
            *(LAS v2u*)(LO + (16 * qt + c16) * 272 + (16 * T + 4 * g) * 2) = st;
        }
    }
    const auto rO = mk_rsrc(O, (unsigned)S_LEN * HD * 2);
#pragma unroll
    for (int i = 0; i < 8; ++i) {
        const int u = i * 64 + l, r = u >> 4, ch = u & 15;
        st16_sc1(*(const LAS v4u*)(LO + r * 272 + ch * 16), rO, (unsigned)((rowb + r) * HD + h * DH + ch * 8) * 2u);
    }
}

constexpr int NBULK = 224;
#define NRB_IN 12
struct Params {
    const u16 *x, *mem0, *Wq, *Wk, *Wv, *Wo, *wg, *wu, *wd;
    u16* out; unsigned char* ws;
};
__device__ __forceinline__ void bulk_wait(const unsigned* p, unsigned want) {
    if (threadIdx.x < 64) {
        unsigned spins = 0;
        while ((unsigned)__builtin_amdgcn_readfirstlane(ld_flag(p)) < want) { __builtin_amdgcn_s_sleep(48); if (++spins > SPIN_CAP) break; }
    }
    __syncthreads();
    __builtin_amdgcn_fence(__ATOMIC_ACQUIRE, "agent");
    asm volatile("s_waitcnt vmcnt(0)" ::: "memory");
}
__device__ __forceinline__ void bulk_arrive(unsigned* p) {
    asm volatile("s_waitcnt vmcnt(0)" ::: "memory");
    __syncthreads();
    if (threadIdx.x == 0) __hip_atomic_fetch_add(p, 1u, __ATOMIC_RELAXED, __HIP_MEMORY_SCOPE_AGENT);
}
__global__ void __launch_bounds__(256, 1) k_fused(Params P) {
    extern __shared__ __attribute__((aligned(16))) unsigned char smem[];
    unsigned char* ws = P.ws;
    u16* xn = (u16*)(ws + WS_XN); u16* Q = (u16*)(ws + WS_Q); u16* Kx = (u16*)(ws + WS_K); u16* Vx = (u16*)(ws + WS_V);
    u16* O = (u16*)(ws + WS_O); u16* kmem = (u16*)(ws + WS_KM); u16* vmem = (u16*)(ws + WS_VM);
    if (blockIdx.x < NH) {
        chain_body(smem, P.Wq, P.Wk, P.Wv, P.Wo, P.mem0, Kx, Vx, kmem, vmem, ws);
        return;
    }
    u16* H = (u16*)(ws + WS_H); u16* HN = (u16*)(ws + WS_HN); u16* ACT = (u16*)(ws + WS_ACT);
    unsigned* flags = (unsigned*)(ws + WS_FLAGS);
    const int b = blockIdx.x - NH, t = threadIdx.x, w = __builtin_amdgcn_readfirstlane(t >> 6), l = t & 63;
    for (int i = b; i < 16 * 96; i += NBULK) {
        int rb, hh, mat;
        if (i < 1024) { rb = i >> 6; hh = (i >> 1) & 31; mat = 1 + (i & 1); }
        else { const int j = i - 1024; rb = j >> 5; hh = j & 31; mat = 0; }
        const u16* W = mat == 0 ? P.Wq : (mat == 1 ? P.Wk : P.Wv);
        u16* C = mat == 0 ? Q : (mat == 1 ? Kx : Vx);
        gemm_tile<0, true, 128, 128>(smem, xn, DM, W, nullptr, HD, C, HD, nullptr, DM, rb * 128, hh * DH);
        asm volatile("s_waitcnt vmcnt(0)" ::: "memory");
        __syncthreads();
        if (t == 0) __hip_atomic_fetch_add(flags + FL_QKV + (rb * 32 + hh) * 16, mat == 0 ? 4u : 1u, __ATOMIC_RELAXED, __HIP_MEMORY_SCOPE_AGENT);
    }
    float* slab = (float*)(ws + WS_SLAB);
    const int role = b < 48 ? 0 : (b < 112 ? 1 : (b < 128 ? 2 : (b < 192 ? 3 : 4)));
    const int rb0 = role == 0 ? 0 : (role == 1 ? 48 : (role == 2 ? 112 : (role == 3 ? 128 : 192)));
    const int nrole = role == 0 ? 48 : (role == 1 ? 64 : (role == 2 ? 16 : (role == 3 ? 64 : 32)));
    const int ntask = role == 0 ? 32 : (role == 1 ? 16 : (role == 2 ? 16 : (role == 3 ? 28 : 8)));
    const int nrb = role == 0 ? 16 : NRB_IN;
    for (int idx = b - rb0; idx < nrb * ntask; idx += nrole) {
        const int rb = idx / ntask, r = idx % ntask, m0 = rb * 128;
        unsigned* gf = flags + FL_GRP + rb * 4 * 16;
        if (role == 0) {
            const int s = 4 * rb + (r >> 3), hh = (r & 7) * 4 + w;
            wait_flag_ge(flags + FL_QKV + (rb * 32 + hh) * 16, 6, 32);
            __builtin_amdgcn_fence(__ATOMIC_ACQUIRE, "agent");
            asm volatile("s_waitcnt vmcnt(0)" ::: "memory");
            __syncthreads();
            xattn_task(smem, Q, Kx, Vx, kmem, vmem, O, s, hh, flags + FL_PROG);
            bulk_arrive(gf + 0 * 16);
        } else if (role == 1) {
            const int ks = r >> 2, nt = r & 3;
            bulk_wait(gf + 0 * 16, 32);
            gemm_tile<4, true, 128, 128>(smem, O + ks * 1024, HD, P.Wo + (size_t)ks * 1024 * DM, nullptr, DM, (u16*)(slab + (size_t)ks * S_LEN * DM), DM, nullptr, 1024, m0, nt * 128);
            bulk_arrive(gf + 1 * 16);
        } else if (role == 2) {
            bulk_wait(gf + 1 * 16, 16);
#pragma unroll
            for (int rr = 0; rr < 2; ++rr) {
                const int row = m0 + r * 8 + w * 2 + rr;
                f32x4 a0 = (f32x4){0.f, 0.f, 0.f, 0.f}, a1 = a0;
#pragma unroll
                for (int ks = 0; ks < 4; ++ks) {
                    const float* src = slab + ((size_t)ks * S_LEN + row) * DM + l * 8;
                    a0 += *(const f32x4*)src; a1 += *(const f32x4*)(src + 4);
                }
                const v4u xr = *(const v4u*)(P.x + (size_t)row * DM + l * 8);
                float v[8];
#pragma unroll
                for (int i = 0; i < 4; ++i) {
                    const float lo = i < 2 ? a0[2 * i] : a1[2 * i - 4], hi = i < 2 ? a0[2 * i + 1] : a1[2 * i - 3];
                    v[2 * i] = rbf(__uint_as_float(xr[i] << 16) + rbf(lo)); v[2 * i + 1] = rbf(__uint_as_float(xr[i] & 0xffff0000u) + rbf(hi));
                }
                float sm = 0.f;
#pragma unroll
                for (int i = 0; i < 8; ++i) sm += v[i];
#pragma unroll
                for (int o = 32; o >= 1; o >>= 1) sm += __shfl_xor(sm, o);
                const float mean = sm * (1.0f / DM);
                float q = 0.f;
#pragma unroll
                for (int i = 0; i < 8; ++i) { const float d = v[i] - mean; q += d * d; }
#pragma unroll
                for (int o = 32; o >= 1; o >>= 1) q += __shfl_xor(q, o);
                const float mb = rbf(mean), den = rbf(sqrtf(rbf(rbf(q * (1.0f / DM)) + 1e-5f)));
                v2u h0 = {pk2(v[0], v[1]), pk2(v[2], v[3])}, h1 = {pk2(v[4], v[5]), pk2(v[6], v[7])};
                v2u n0 = {pk2(rbf(v[0] - mb) / den, rbf(v[1] - mb) / den), pk2(rbf(v[2] - mb) / den, rbf(v[3] - mb) / den)};
                v2u n1 = {pk2(rbf(v[4] - mb) / den, rbf(v[5] - mb) / den), pk2(rbf(v[6] - mb) / den, rbf(v[7] - mb) / den)};
                st8_dev(H + (size_t)row * DM + l * 8, h0); st8_dev(H + (size_t)row * DM + l * 8 + 4, h1);
                st8_dev(HN + (size_t)row * DM + l * 8, n0); st8_dev(HN + (size_t)row * DM + l * 8 + 4, n1);
            }
            bulk_arrive(gf + 2 * 16);
        } else if (role == 3) {
            bulk_wait(gf + 2 * 16, 16);
            gemm_tile<2, true, 128, 128>(smem, HN, DM, P.wg, P.wu, FF, ACT, FF, nullptr, DM, m0, r * 64);
            bulk_arrive(gf + 3 * 16);
        } else {
            bulk_wait(gf + 3 * 16, 28);
            gemm_tile<1, false, 128, 64>(smem, ACT, FF, P.wd, nullptr, DM, P.out, DM, H, FF, m0, r * 64);
        }
    }
}

extern "C" void kernel_launch(void* const* d_in, const int* in_sizes, int n_in, void* d_out, int out_size, void* d_ws, size_t ws_size,
                              hipStream_t stream) {
    const u16* x = (const u16*)d_in[0];
    const u16* mem0 = (const u16*)d_in[1];
    const u16* Wq = (const u16*)d_in[2];
    const u16* Wk = (const u16*)d_in[3];
    const u16* Wv = (const u16*)d_in[4];
    const u16* Wo = (const u16*)d_in[5];
    const u16* wg = (const u16*)d_in[8];
    const u16* wu = (const u16*)d_in[9];
    const u16* wd = (const u16*)d_in[10];
    u16* out = (u16*)d_out;
    unsigned char* ws = (unsigned char*)d_ws;
    u16* xn = (u16*)(ws + WS_XN); u16* Q = (u16*)(ws + WS_Q); u16* K = (u16*)(ws + WS_K); u16* V = (u16*)(ws + WS_V);
    u16* O = (u16*)(ws + WS_O); u16* km = (u16*)(ws + WS_KM); u16* vm = (u16*)(ws + WS_VM);
    u16* H = (u16*)(ws + WS_H); u16* HN = (u16*)(ws + WS_HN); u16* ACT = (u16*)(ws + WS_ACT);

    (void)hipFuncSetAttribute((const void*)k_fused, hipFuncAttributeMaxDynamicSharedMemorySize, C_LDS);
#define TBM 64
#define TBN 64
#define TBM1 64
    (void)hipFuncSetAttribute((const void*)k_gemm<2, TBM1, 128>, hipFuncAttributeMaxDynamicSharedMemorySize, 2 * g_stage(TBM1, 128));
    (void)hipFuncSetAttribute((const void*)k_gemm_splitk<TBM, TBN>, hipFuncAttributeMaxDynamicSharedMemorySize, 2 * g_stage(TBM, TBN));
    k_ln<true><<<S_LEN / 4, 256, 0, stream>>>(x, xn, ws);
    Params P{x, mem0, Wq, Wk, Wv, Wo, wg, wu, wd, out, ws};
    k_fused<<<NH + NBULK, 256, C_LDS, stream>>>(P);
    float* slab = (float*)(ws + WS_SLAB);
    constexpr int R0 = NRB_IN * 128, NR = S_LEN - R0;
    k_gemm_splitk<TBM, TBN><<<dim3(DM / TBN, NR / TBM, 8), 256, 2 * g_stage(TBM, TBN), stream>>>(O, HD, Wo, DM, slab, HD / 8, R0);
    k_reduce<8, true><<<NR / 4, 256, 0, stream>>>(slab, x, H, HN, R0);
    k_gemm<2, TBM1, 128><<<dim3(FF / 64, NR / TBM1), 256, 2 * g_stage(TBM1, 128), stream>>>(HN, DM, wg, wu, FF, ACT, FF, nullptr, DM, R0);
    k_gemm_splitk<TBM, TBN><<<dim3(DM / TBN, NR / TBM, 7), 256, 2 * g_stage(TBM, TBN), stream>>>(ACT, FF, wd, DM, slab, FF / 7, R0);
    k_reduce<7, false><<<NR / 4, 256, 0, stream>>>(slab, H, out, nullptr, R0);
}
```

```cpp
#include <hip/hip_runtime.h>
#include <hip/hip_bf16.h>
#include <stdint.h>

typedef unsigned short u16;
typedef __bf16 bf16x8 __attribute__((ext_vector_type(8)));
typedef __bf16 bf16x4 __attribute__((ext_vector_type(4)));
typedef __bf16 bf16x16 __attribute__((ext_vector_type(16)));
typedef short s16x4 __attribute__((ext_vector_type(4)));
typedef float f32x4 __attribute__((ext_vector_type(4)));
typedef unsigned v4u __attribute__((ext_vector_type(4)));
typedef unsigned v2u __attribute__((ext_vector_type(2)));
#define LAS __attribute__((address_space(3)))

constexpr int S_LEN = 2048, DM = 512, NH = 32, DH = 128, HD = NH * DH, MM = 8, SEG = 32, NSEG = S_LEN / SEG, FF = 1792;

constexpr size_t WS_RS = 0;
constexpr size_t WS_AG = WS_RS + 2u * 32 * 32 * 512;
constexpr size_t WS_SYNC_END = WS_AG + 2u * 8192;
constexpr size_t WS_FLAGS = WS_SYNC_END;
constexpr int FL_QKV = 0;
constexpr int FL_PROG = 16 * 32 * 16;
constexpr int FL_GRP = FL_PROG + 64;
constexpr int FL_WORDS = FL_GRP + 16 * 4 * 16;
constexpr size_t WS_FLAGS_END = WS_FLAGS + FL_WORDS * 4;
constexpr size_t WS_XN = 2u << 20;
constexpr size_t WS_Q = WS_XN + (size_t)S_LEN * DM * 2;
constexpr size_t WS_K = WS_Q + (size_t)S_LEN * HD * 2;
constexpr size_t WS_V = WS_K + (size_t)S_LEN * HD * 2;
constexpr size_t WS_O = WS_V + (size_t)S_LEN * HD * 2;
constexpr size_t WS_KM = WS_O + (size_t)S_LEN * HD * 2;
constexpr size_t WS_VM = WS_KM + (size_t)NSEG * NH * MM * DH * 2;
constexpr size_t WS_H = WS_VM + (size_t)NSEG * NH * MM * DH * 2;
constexpr size_t WS_HN = WS_H + (size_t)S_LEN * DM * 2;
constexpr size_t WS_ACT = WS_HN + (size_t)S_LEN * DM * 2;
constexpr size_t WS_SLAB = WS_ACT + (size_t)S_LEN * FF * 2;
constexpr size_t WS_END = WS_SLAB + (size_t)8 * S_LEN * DM * 4;

__device__ __forceinline__ float bf2f(u16 v) { return __uint_as_float((unsigned)v << 16); }
__device__ __forceinline__ u16 f2bf(float f) { return __builtin_bit_cast(u16, (__bf16)f); }
__device__ __forceinline__ float rbf(float f) { return (float)(__bf16)f; }
typedef __bf16 bf16x2 __attribute__((ext_vector_type(2)));
typedef float f32x2 __attribute__((ext_vector_type(2)));
__device__ __forceinline__ unsigned pk2(float lo, float hi) {
    const f32x2 v = {lo, hi};
    return __builtin_bit_cast(unsigned, __builtin_convertvector(v, bf16x2));
}
__device__ __forceinline__ float xl32_add(float v) { auto r = __builtin_amdgcn_permlane32_swap(__float_as_uint(v), __float_as_uint(v), false, false); return __uint_as_float(r[0]) + __uint_as_float(r[1]); }
__device__ __forceinline__ float xl16_add(float v) { auto r = __builtin_amdgcn_permlane16_swap(__float_as_uint(v), __float_as_uint(v), false, false); return __uint_as_float(r[0]) + __uint_as_float(r[1]); }
__device__ __forceinline__ float xl32_max(float v) { auto r = __builtin_amdgcn_permlane32_swap(__float_as_uint(v), __float_as_uint(v), false, false); return fmaxf(__uint_as_float(r[0]), __uint_as_float(r[1])); }
__device__ __forceinline__ float xl16_max(float v) { auto r = __builtin_amdgcn_permlane16_swap(__float_as_uint(v), __float_as_uint(v), false, false); return fmaxf(__uint_as_float(r[0]), __uint_as_float(r[1])); }
__device__ __forceinline__ float xl8_add(float v) { return v + __uint_as_float(__builtin_amdgcn_update_dpp(0, __float_as_uint(v), 0x128, 0xf, 0xf, false)); }
__device__ __forceinline__ float xl1_get(float v) { return __uint_as_float(__builtin_amdgcn_update_dpp(0, __float_as_uint(v), 0xB1, 0xf, 0xf, false)); }
#define SBAR() __builtin_amdgcn_sched_barrier(0)
__device__ __forceinline__ s16x4 tr_read(const LAS unsigned char* p) {
    return __builtin_amdgcn_ds_read_tr16_b64_v4i16((LAS s16x4*)p);
}
__device__ __forceinline__ bf16x8 cat8(s16x4 a, s16x4 b) {
    typedef short s16x8 __attribute__((ext_vector_type(8)));
    s16x8 r = __builtin_shufflevector(a, b, 0, 1, 2, 3, 4, 5, 6, 7);
    return __builtin_bit_cast(bf16x8, r);
}
__device__ __forceinline__ bf16x16 cat16(bf16x8 a, bf16x8 b) {
    return __builtin_shufflevector(a, b, 0, 1, 2, 3, 4, 5, 6, 7, 8, 9, 10, 11, 12, 13, 14, 15);
}
#define MFMA32(a, b, c) __builtin_amdgcn_mfma_f32_16x16x32_bf16((a), (b), (c), 0, 0, 0)
#define SMFMAC(a, b, c, idx) __builtin_amdgcn_smfmac_f32_16x16x64_bf16((a), (b), (c), (idx), 0, 0)

template <bool INIT>
__global__ void __launch_bounds__(256) k_ln(const u16* __restrict__ x, u16* __restrict__ y, unsigned char* __restrict__ ws) {
    const int row = blockIdx.x * 4 + (threadIdx.x >> 6), lane = threadIdx.x & 63;
    if (INIT) {
        const size_t u = (size_t)blockIdx.x * 256 + threadIdx.x;
        if (u * 16 < WS_SYNC_END) *(v4u*)(ws + u * 16) = (v4u){0xFFFFFFFFu, 0xFFFFFFFFu, 0xFFFFFFFFu, 0xFFFFFFFFu};
        else if (u * 16 < WS_FLAGS_END) *(v4u*)(ws + u * 16) = (v4u){0u, 0u, 0u, 0u};
    }
    const v4u raw = *(const v4u*)(x + (size_t)row * DM + lane * 8);
    float v[8];
#pragma unroll
    for (int i = 0; i < 4; ++i) { v[2 * i] = __uint_as_float(raw[i] << 16); v[2 * i + 1] = __uint_as_float(raw[i] & 0xffff0000u); }
    float s = 0.f;
#pragma unroll
    for (int i = 0; i < 8; ++i) s += v[i];
#pragma unroll
    for (int o = 32; o >= 1; o >>= 1) s += __shfl_xor(s, o);
    const float mean = s * (1.0f / DM);
    float q = 0.f;
#pragma unroll
    for (int i = 0; i < 8; ++i) { const float d = v[i] - mean; q += d * d; }
#pragma unroll
    for (int o = 32; o >= 1; o >>= 1) q += __shfl_xor(q, o);
    const float mb = rbf(mean), vb = rbf(q * (1.0f / DM));
    const float den = rbf(sqrtf(rbf(vb + 1e-5f)));
    v4u o;
#pragma unroll
    for (int i = 0; i < 4; ++i) o[i] = pk2(rbf(v[2 * i] - mb) / den, rbf(v[2 * i + 1] - mb) / den);
    *(v4u*)(y + (size_t)row * DM + lane * 8) = o;
}

constexpr int G_BK = 64, G_AST = 144;
__host__ __device__ constexpr int g_stage(int BM, int BN) { return BM * G_AST + G_BK * (BN * 2 + 32); }
__device__ __forceinline__ void st8_dev(u16* p, v2u v) {
    __hip_atomic_store((unsigned long long*)p, (unsigned long long)v[0] | ((unsigned long long)v[1] << 32), __ATOMIC_RELAXED, __HIP_MEMORY_SCOPE_AGENT);
}
template <int EPI, bool DEV, int BM, int BN>
__device__ __forceinline__ void gemm_tile(unsigned char* smem, const u16* __restrict__ A, int lda, const u16* __restrict__ B, const u16* __restrict__ B2, int ldb,
                                          u16* __restrict__ C, int ldc, const u16* __restrict__ res, int K, int m0, int n0) {
    constexpr int BST = BN * 2 + 32, ABYTES = BM * G_AST, STAGE = g_stage(BM, BN);
    constexpr int NA = BM / 32, NB = BN / 32, MI = BM / 32, NJ = BN / 32, BCH = BN / 8;
    const int t = threadIdx.x, w = t >> 6, l = t & 63, g = l >> 4, c16 = l & 15;
    const int wr = w >> 1, wc = w & 1;
    v4u ra[4][NA], rb[4][NB];
    auto gload = [&](int k0, v4u (&da)[NA], v4u (&db)[NB]) {
#pragma unroll
        for (int i = 0; i < NA; ++i) { const int u = i * 256 + t; da[i] = *(const v4u*)(A + (size_t)(m0 + (u >> 3)) * lda + k0 + (u & 7) * 8); }
#pragma unroll
        for (int i = 0; i < NB; ++i) {
            const int u = i * 256 + t, kr = u / BCH, ch = u % BCH;
            if (EPI == 2) {
                constexpr int Q = BCH / 4;
                const int wcc = ch / (2 * Q), part = (ch / Q) & 1, cc = (ch % Q) * 8;
                db[i] = *(const v4u*)((part ? B2 : B) + (size_t)(k0 + kr) * ldb + n0 + (BN / 4) * wcc + cc);
            } else db[i] = *(const v4u*)(B + (size_t)(k0 + kr) * ldb + n0 + ch * 8);
        }
    };
    auto swrite = [&](int buf, v4u (&da)[NA], v4u (&db)[NB]) {
        unsigned char* sa = smem + buf * STAGE; unsigned char* sb = sa + ABYTES;
#pragma unroll
        for (int i = 0; i < NA; ++i) { const int u = i * 256 + t; *(v4u*)(sa + (u >> 3) * G_AST + (u & 7) * 16) = da[i]; }
#pragma unroll
        for (int i = 0; i < NB; ++i) { const int u = i * 256 + t; *(v4u*)(sb + (u / BCH) * BST + (u % BCH) * 16) = db[i]; }
    };
    f32x4 acc[MI][NJ];
#pragma unroll
    for (int i = 0; i < MI; ++i)
#pragma unroll
        for (int j = 0; j < NJ; ++j) acc[i][j] = (f32x4){0.f, 0.f, 0.f, 0.f};
    bf16x8 af[2][MI], bfr[2][NJ];
    auto fread = [&](int buf) {
        const LAS unsigned char* sa = (const LAS unsigned char*)smem + buf * STAGE;
        const LAS unsigned char* sb = sa + ABYTES;
#pragma unroll
        for (int ks = 0; ks < 2; ++ks) {
#pragma unroll
            for (int i = 0; i < MI; ++i) af[ks][i] = *(const LAS bf16x8*)(sa + (wr * (BM / 2) + i * 16 + c16) * G_AST + (ks * 32 + g * 8) * 2);
#pragma unroll
            for (int j = 0; j < NJ; ++j) {
                const LAS unsigned char* p = sb + (ks * 32 + g * 8 + (c16 >> 2)) * BST + (wc * (BN / 2) + j * 16 + (c16 & 3) * 4) * 2;
                bfr[ks][j] = cat8(tr_read(p), tr_read(p + 4 * BST));
            }
        }
    };
    auto mma = [&](int ks) {
#pragma unroll
        for (int i = 0; i < MI; ++i)
#pragma unroll
            for (int j = 0; j < NJ; ++j) acc[i][j] = MFMA32(bfr[ks][j], af[ks][i], acc[i][j]);
    };
    const int nk = K / G_BK;
    const int klast = K - G_BK;
#pragma unroll
    for (int i = 0; i < 4; ++i) gload(i * G_BK, ra[i], rb[i]);
    __syncthreads();
    swrite(0, ra[0], rb[0]);
    gload(min(4 * G_BK, klast), ra[0], rb[0]);
    __syncthreads();
    for (int kt = 0; kt < nk; kt += 4) {
#pragma unroll
        for (int u = 0; u < 4; ++u) {
            fread(u & 1);
            mma(0);
            swrite((u + 1) & 1, ra[(u + 1) & 3], rb[(u + 1) & 3]);
            gload(min((kt + u + 5) * G_BK, klast), ra[(u + 1) & 3], rb[(u + 1) & 3]);
            mma(1);
            __syncthreads();
        }
    }
    if (EPI == 4) {
        const auto rC = __builtin_amdgcn_make_buffer_rsrc((void*)C, 0, (unsigned)S_LEN * DM * 4, 0x00020000);
#pragma unroll
        for (int i = 0; i < MI; ++i)
#pragma unroll
            for (int j = 0; j < NJ; ++j)
                __builtin_amdgcn_raw_buffer_store_b128(__builtin_bit_cast(v4u, acc[i][j]), rC, (unsigned)((m0 + wr * (BM / 2) + i * 16 + c16) * ldc + n0 + wc * (BN / 2) + j * 16 + 4 * g) * 4u, 0, DEV ? 16 : 0);
        return;
    }
    constexpr int ON = EPI == 2 ? BN / 2 : BN, OST = ON * 2 + 16;
#pragma unroll
    for (int i = 0; i < MI; ++i) {
        const int m = m0 + wr * (BM / 2) + i * 16 + c16;
        if (EPI == 2) {
#pragma unroll
            for (int j = 0; j < NJ / 2; ++j) {
                float o[4];
#pragma unroll
                for (int r = 0; r < 4; ++r) {
                    const float gt = rbf(acc[i][j][r]), up = rbf(acc[i][j + NJ / 2][r]);
                    const float inner = 0.7978845608028654f * (gt + 0.044715f * gt * gt * gt);
                    const float th = 1.0f - 2.0f * __builtin_amdgcn_rcpf(__expf(2.0f * inner) + 1.0f);
                    const float ge = rbf(0.5f * gt * (1.0f + th));
                    o[r] = ge * up;
                }
                v2u st = {pk2(o[0], o[1]), pk2(o[2], o[3])};
                u16* dst = C + (size_t)m * ldc + n0 + wc * (BN / 4) + j * 16 + 4 * g;
                if (DEV) *(v2u*)(smem + (m - m0) * OST + (wc * (BN / 4) + j * 16 + 4 * g) * 2) = st; else *(v2u*)dst = st;
            }
        } else {
#pragma unroll
            for (int j = 0; j < NJ; ++j) {
                const int n = n0 + wc * (BN / 2) + j * 16 + 4 * g;
                float o[4];
#pragma unroll
                for (int r = 0; r < 4; ++r) o[r] = acc[i][j][r];
                if (EPI == 1) {
                    const v2u rr = *(const v2u*)(res + (size_t)m * ldc + n);
                    o[0] = __uint_as_float(rr[0] << 16) + rbf(o[0]); o[1] = __uint_as_float(rr[0] & 0xffff0000u) + rbf(o[1]);
                    o[2] = __uint_as_float(rr[1] << 16) + rbf(o[2]); o[3] = __uint_as_float(rr[1] & 0xffff0000u) + rbf(o[3]);
                }
                v2u st = {pk2(o[0], o[1]), pk2(o[2], o[3])};
                if (DEV) *(v2u*)(smem + (m - m0) * OST + (n - n0) * 2) = st; else *(v2u*)(C + (size_t)m * ldc + n) = st;
            }
        }
    }
    if (DEV) {
        __syncthreads();
        constexpr int CH = ON / 8;
        const auto rC = __builtin_amdgcn_make_buffer_rsrc((void*)C, 0, 0x7fffffffu, 0x00020000);
#pragma unroll
        for (int i = 0; i < BM * CH / 256; ++i) {
            const int u = i * 256 + t, row = u / CH, ch = u % CH;
            __builtin_amdgcn_raw_buffer_store_b128(*(const v4u*)(smem + row * OST + ch * 16), rC, (unsigned)((m0 + row) * ldc + n0 + ch * 8) * 2u, 0, 16);
        }
    }
}
template <int EPI, int BM, int BN>
__global__ void __launch_bounds__(256) k_gemm(const u16* __restrict__ A, int lda, const u16* __restrict__ B, const u16* __restrict__ B2, int ldb,
                                              u16* __restrict__ C, int ldc, const u16* __restrict__ res, int K, int row0) {
    extern __shared__ __attribute__((aligned(16))) unsigned char smem[];
    gemm_tile<EPI, false, BM, BN>(smem, A, lda, B, B2, ldb, C, ldc, res, K, row0 + blockIdx.y * BM, blockIdx.x * (EPI == 2 ? BN / 2 : BN));
}

template <int BM, int BN>
__global__ void __launch_bounds__(256) k_gemm_splitk(const u16* __restrict__ A, int lda, const u16* __restrict__ B, int ldb, float* __restrict__ slab, int Kpart, int row0) {
    extern __shared__ __attribute__((aligned(16))) unsigned char smem[];
    const int z = blockIdx.z;
    gemm_tile<4, false, BM, BN>(smem, A + (size_t)z * Kpart, lda, B + (size_t)z * Kpart * ldb, nullptr, ldb, (u16*)(slab + (size_t)z * S_LEN * DM), DM, nullptr, Kpart, row0 + blockIdx.y * BM, blockIdx.x * BN);
}
template <int NS, bool LN>
__global__ void __launch_bounds__(256) k_reduce(const float* __restrict__ slab, const u16* __restrict__ res, u16* __restrict__ hout, u16* __restrict__ nout, int row0) {
    const int row = row0 + blockIdx.x * 4 + (threadIdx.x >> 6), l = threadIdx.x & 63;
    f32x4 a0 = (f32x4){0.f, 0.f, 0.f, 0.f}, a1 = a0;
#pragma unroll
    for (int ks = 0; ks < NS; ++ks) {
        const float* src = slab + ((size_t)ks * S_LEN + row) * DM + l * 8;
        a0 += *(const f32x4*)src; a1 += *(const f32x4*)(src + 4);
    }
    const v4u xr = *(const v4u*)(res + (size_t)row * DM + l * 8);
    float v[8];
#pragma unroll
    for (int i = 0; i < 4; ++i) {
        const float lo = i < 2 ? a0[2 * i] : a1[2 * i - 4], hi = i < 2 ? a0[2 * i + 1] : a1[2 * i - 3];
        v[2 * i] = rbf(__uint_as_float(xr[i] << 16) + rbf(lo)); v[2 * i + 1] = rbf(__uint_as_float(xr[i] & 0xffff0000u) + rbf(hi));
    }
    *(v4u*)(hout + (size_t)row * DM + l * 8) = (v4u){pk2(v[0], v[1]), pk2(v[2], v[3]), pk2(v[4], v[5]), pk2(v[6], v[7])};
    if (LN) {
        float sm = 0.f;
#pragma unroll
        for (int i = 0; i < 8; ++i) sm += v[i];
#pragma unroll
        for (int o = 32; o >= 1; o >>= 1) sm += __shfl_xor(sm, o);
        const float mean = sm * (1.0f / DM);
        float q = 0.f;
#pragma unroll
        for (int i = 0; i < 8; ++i) { const float d = v[i] - mean; q += d * d; }
#pragma unroll
        for (int o = 32; o >= 1; o >>= 1) q += __shfl_xor(q, o);
        const float mb = rbf(mean), den = rbf(sqrtf(rbf(rbf(q * (1.0f / DM)) + 1e-5f)));
        v4u o;
#pragma unroll
        for (int i = 0; i < 4; ++i) o[i] = pk2(rbf(v[2 * i] - mb) / den, rbf(v[2 * i + 1] - mb) / den);
        *(v4u*)(nout + (size_t)row * DM + l * 8) = o;
    }
}

constexpr unsigned SENT = 0xFFFFFFFFu;
constexpr int C_WV = 0;
constexpr int C_MEM = 131072;
constexpr int RST = 544;
constexpr int C_QS = C_MEM + 8192;
constexpr int C_KS = C_QS + 256;
constexpr int C_VS = C_QS + 8 * RST;
constexpr int C_KX = C_VS + 2048;
constexpr int C_VX = C_KX + 256;
constexpr int C_LDS = C_KX + 32 * RST;
constexpr unsigned SPIN_CAP = 1u << 22;
#define DLY1 14
#define DLY2 12

__device__ __forceinline__ __amdgpu_buffer_rsrc_t mk_rsrc(const void* p, unsigned bytes) {
    return __builtin_amdgcn_make_buffer_rsrc((void*)p, 0, bytes, 0x00020000);
}
__device__ __forceinline__ v4u ld16_sc1(__amdgpu_buffer_rsrc_t r, unsigned off) { return __builtin_amdgcn_raw_buffer_load_b128(r, off, 0, 16); }
__device__ __forceinline__ void st16_sc1(v4u v, __amdgpu_buffer_rsrc_t r, unsigned off) { __builtin_amdgcn_raw_buffer_store_b128(v, r, off, 0, 16); }
__device__ __forceinline__ void st4_sc1(unsigned v, __amdgpu_buffer_rsrc_t r, unsigned off) { __builtin_amdgcn_raw_buffer_store_b32(v, r, off, 0, 16); }
__device__ __forceinline__ void dly(int n) { for (int i = 0; i < n; ++i) __builtin_amdgcn_s_sleep(1); }

__device__ __forceinline__ f32x4 comb2(f32x4 a, f32x4 b) {
    f32x4 o;
#pragma unroll
    for (int r = 0; r < 4; ++r) {
        auto sw = __builtin_amdgcn_permlane32_swap(__float_as_uint(a[r]), __float_as_uint(b[r]), false, false);
        o[r] = __uint_as_float(sw[0]) + __uint_as_float(sw[1]);
    }
    return o;
}

__device__ __forceinline__ unsigned ld_flag(const unsigned* p) { return __hip_atomic_load(p, __ATOMIC_RELAXED, __HIP_MEMORY_SCOPE_AGENT); }
__device__ __forceinline__ void wait_flag_ge(const unsigned* p, unsigned want, int sleep_n) {
    unsigned spins = 0;
    while ((unsigned)__builtin_amdgcn_readfirstlane(ld_flag(p)) < want) { dly(sleep_n); if (++spins > SPIN_CAP) break; }
}
__device__ __forceinline__ void wait_flag_kv(const unsigned* p) {
    unsigned spins = 0;
    while (((unsigned)__builtin_amdgcn_readfirstlane(ld_flag(p)) & 3u) != 2u) { dly(2); if (++spins > SPIN_CAP) break; }
}
__device__ __forceinline__ void chain_body(unsigned char* smem, const u16* __restrict__ Wq, const u16* __restrict__ Wk, const u16* __restrict__ Wv,
                                           const u16* __restrict__ Wo, const u16* __restrict__ mem0,
                                           const u16* Kx, const u16* Vx, u16* kmem, u16* vmem, unsigned char* ws_sync) {
    LAS unsigned char* L = (LAS unsigned char*)smem;
    unsigned* flags = (unsigned*)(ws_sync + WS_FLAGS);
    const auto rK = mk_rsrc(Kx, (unsigned)S_LEN * HD * 2);
    const auto rV = mk_rsrc(Vx, (unsigned)S_LEN * HD * 2);
    const auto rKM = mk_rsrc(kmem, (unsigned)NSEG * NH * MM * DH * 2);
    const auto rVM = mk_rsrc(vmem, (unsigned)NSEG * NH * MM * DH * 2);
    const int h = blockIdx.x, t = threadIdx.x, w = __builtin_amdgcn_readfirstlane(t >> 6), l = t & 63, g = l >> 4, c16 = l & 15;
    const auto rRS = mk_rsrc(ws_sync + WS_RS, 2u * 32 * 32 * 512);
    const auto rAG = mk_rsrc(ws_sync + WS_AG, 2u * 8192);
    const int sidx = (c16 < 8) ? 0x4444 : 0xEEEE;

    bf16x16 Bq[2][8], Bk[2][8], Bo[8][2];
    {
        constexpr int ST = C_MEM;
        v4u P[6][4];
        auto gl = [&](int r, v4u (&d)[4]) {
#pragma unroll
            for (int i = 0; i < 4; ++i) {
                const int u = i * 256 + t, k = u >> 4, ch = u & 15;
                if (r < 24) {
                    const u16* W = r < 8 ? Wq : (r < 16 ? Wk : Wv);
                    d[i] = *(const v4u*)(W + (size_t)(64 * (r & 7) + k) * HD + h * DH + ch * 8);
                } else {
                    const int kk = (r - 24) >> 2, cb = (r - 24) & 3;
                    const int gg = k >> 4, wi = k & 15, ii = 2 * (wi >> 2) + (wi & 1), cc = (wi >> 1) & 1;
                    const int e = 64 * kk + 32 * cc + 8 * gg + ii;
                    d[i] = *(const v4u*)(Wo + ((size_t)h * DH + e) * DM + cb * 128 + ch * 8);
                }
            }
        };
#pragma unroll
        for (int r = 0; r < 6; ++r) gl(r, P[r]);
#pragma unroll
        for (int r = 0; r < 32; ++r) {
            __syncthreads();
#pragma unroll
            for (int i = 0; i < 4; ++i) { const int u = i * 256 + t; *(LAS v4u*)(L + ST + (u >> 4) * 288 + (u & 15) * 16) = P[r % 6][i]; }
            if (r + 6 < 32) gl(r + 6, P[r % 6]);
            __syncthreads();
            if (r < 24) {
                const int mat = r >> 3, ks = r & 7;
#pragma unroll
                for (int tt = 0; tt < 2; ++tt) {
                    const LAS unsigned char* p = L + ST + (8 * g + (c16 >> 2)) * 288 + (32 * w + 16 * tt + (c16 & 3) * 4) * 2;
                    const bf16x8 lo = cat8(tr_read(p), tr_read(p + 4 * 288));
                    const bf16x8 hi = cat8(tr_read(p + 32 * 288), tr_read(p + 36 * 288));
                    if (mat == 0) Bq[tt][ks] = cat16(lo, hi);
                    else if (mat == 1) Bk[tt][ks] = cat16(lo, hi);
                    else {
                        *(LAS bf16x8*)(L + C_WV + (((2 * w + tt) * 8 + ks) * 2 + 0) * 1024 + l * 16) = lo;
                        *(LAS bf16x8*)(L + C_WV + (((2 * w + tt) * 8 + ks) * 2 + 1) * 1024 + l * 16) = hi;
                    }
                }
            } else {
                const int kk = (r - 24) >> 2, cb = (r - 24) & 3;
                if (w == cb) {
#pragma unroll
                    for (int tt = 0; tt < 8; ++tt) {
                        const LAS unsigned char* p = L + ST + (8 * g + (c16 >> 2)) * 288 + (16 * tt + (c16 & 3) * 4) * 2;
                        const bf16x8 lo = cat8(tr_read(p), tr_read(p + 4 * 288));
                        const bf16x8 hi = cat8(tr_read(p + 32 * 288), tr_read(p + 36 * 288));
                        Bo[tt][kk] = cat16(lo, hi);
                    }
                }
            }
        }
        __syncthreads();
    }
    {
#pragma unroll
        for (int i = 0; i < 2; ++i) {
            const int u = i * 256 + t, ks = u >> 6, ll = u & 63, gg = ll >> 4, cc = (ll >> 3) & 1, m = ll & 7;
            const u16* src = mem0 + m * DM + 64 * ks + 16 * gg + 2 * cc;
            v4u v;
#pragma unroll
            for (int q = 0; q < 4; ++q) v[q] = *(const unsigned*)(src + 4 * q);
            *(LAS v4u*)(L + C_MEM + u * 16) = v;
        }
        wait_flag_kv(flags + FL_QKV + h * 16);
#pragma unroll
        for (int i = 0; i < 2; ++i) {
            const int u = i * 256 + t, r = u >> 4, ch = u & 15;
            *(LAS v4u*)(L + C_KX + r * RST + ch * 16) = ld16_sc1(rK, (unsigned)(r * HD + h * DH + ch * 8) * 2u);
            *(LAS v4u*)(L + C_VX + r * RST + ch * 16) = ld16_sc1(rV, (unsigned)(r * HD + h * DH + ch * 8) * 2u);
        }
    }
    __syncthreads();

#define PIN(x) asm volatile("" : "+v"(x))
    for (int s = 0; s < NSEG; ++s) {
        int t = threadIdx.x; PIN(t);
        const int l = t & 63, g = l >> 4, c16 = l & 15;
        unsigned oWV = (unsigned)(size_t)(L + C_WV + (2 * w * 16) * 1024 + l * 16);
        unsigned oMEM = (unsigned)(size_t)(L + C_MEM + l * 16);
        unsigned oKX = (unsigned)(size_t)(L + C_KX + c16 * RST + 16 * g);
        unsigned oQK = (unsigned)(size_t)(L + C_QS + (c16 & 7) * RST + 16 * g);
        unsigned oQW = (unsigned)(size_t)(L + C_QS + 4 * (g & 1) * RST + (32 * w + 16 * (l >> 5) + c16) * 2);
        unsigned oVW = (unsigned)(size_t)(L + C_VS + (32 * w + 16 * (l >> 5) + c16) * 16 + (g & 1) * 8);
        PIN(oWV); PIN(oMEM); PIN(oKX); PIN(oQK); PIN(oQW); PIN(oVW);
#define LP(o) ((LAS unsigned char*)(size_t)(o))
        f32x4 aq[2], ak[2], av[2];
#pragma unroll
        for (int i = 0; i < 2; ++i) { aq[i] = (f32x4){0.f, 0.f, 0.f, 0.f}; ak[i] = aq[i]; av[i] = aq[i]; PIN(aq[i]); PIN(ak[i]); PIN(av[i]); }
        bf16x8 A[8], Vb[2][4];
#pragma unroll
        for (int i = 0; i < 4; ++i) Vb[0][i] = *(const LAS bf16x8*)(LP(oWV) + ((i >> 1) * 16 + (i & 1)) * 1024);
        if (s > 0) {
            const unsigned slot = (s - 1) & 1;
            dly(DLY2);
            v4u x[2];
            unsigned spins = 0;
            for (;;) {
                asm volatile("" ::: "memory");
                bool ok = true;
#pragma unroll
                for (int i = 0; i < 2; ++i) x[i] = ld16_sc1(rAG, slot * 8192u + (i * 256 + t) * 16u);
#pragma unroll
                for (int i = 0; i < 2; ++i) ok &= (x[i][0] != SENT) & (x[i][1] != SENT) & (x[i][2] != SENT) & (x[i][3] != SENT);
                if (__all(ok)) break;
                if (++spins > SPIN_CAP) break;
            }
#pragma unroll
            for (int i = 0; i < 2; ++i) *(LAS v4u*)(LP(oMEM) + (i * 256 + w * 64) * 16) = x[i];
            __syncthreads();
            if (t == 0) __hip_atomic_fetch_add(flags + FL_PROG, 1u, __ATOMIC_RELAXED, __HIP_MEMORY_SCOPE_AGENT);
        }
#pragma unroll
        for (int ks = 0; ks < 2; ++ks) A[ks] = *(const LAS bf16x8*)(LP(oMEM) + ks * 1024);
        SBAR();
#pragma unroll
        for (int ks = 0; ks < 8; ++ks) {
            if (ks < 6) A[ks + 2] = *(const LAS bf16x8*)(LP(oMEM) + (ks + 2) * 1024);
            if (ks + 1 < 8) {
#pragma unroll
                for (int i = 0; i < 4; ++i) Vb[(ks + 1) & 1][i] = *(const LAS bf16x8*)(LP(oWV) + ((i >> 1) * 16 + (ks + 1) * 2 + (i & 1)) * 1024);
            }
            SBAR();
            aq[0] = SMFMAC(A[ks], Bq[0][ks], aq[0], sidx);
            aq[1] = SMFMAC(A[ks], Bq[1][ks], aq[1], sidx);
            ak[0] = SMFMAC(A[ks], Bk[0][ks], ak[0], sidx);
            ak[1] = SMFMAC(A[ks], Bk[1][ks], ak[1], sidx);
            av[0] = SMFMAC(A[ks], cat16(Vb[ks & 1][0], Vb[ks & 1][1]), av[0], sidx);
            av[1] = SMFMAC(A[ks], cat16(Vb[ks & 1][2], Vb[ks & 1][3]), av[1], sidx);
            SBAR();
        }
        bf16x8 kx[2][4];
#pragma unroll
        for (int kq = 0; kq < 4; ++kq) {
            kx[0][kq] = *(const LAS bf16x8*)(LP(oKX) + 64 * kq);
            kx[1][kq] = *(const LAS bf16x8*)(LP(oKX) + 16 * RST + 64 * kq);
        }
        {
            const f32x4 q = comb2(aq[0], aq[1]), k = comb2(ak[0], ak[1]), v = comb2(av[0], av[1]);
            const unsigned q01 = pk2(q[0], q[1]), q23 = pk2(q[2], q[3]), k01 = pk2(k[0], k[1]), k23 = pk2(k[2], k[3]);
            LAS unsigned char* pq = LP(oQW);
            LAS unsigned char* pk = LP(oQW) + (C_KS - C_QS);
            *(LAS u16*)(pq) = (u16)q01; *(LAS u16*)(pq + RST) = (u16)(q01 >> 16); *(LAS u16*)(pq + 2 * RST) = (u16)q23; *(LAS u16*)(pq + 3 * RST) = (u16)(q23 >> 16);
            *(LAS u16*)(pk) = (u16)k01; *(LAS u16*)(pk + RST) = (u16)(k01 >> 16); *(LAS u16*)(pk + 2 * RST) = (u16)k23; *(LAS u16*)(pk + 3 * RST) = (u16)(k23 >> 16);
            v2u vv = {pk2(v[0], v[1]), pk2(v[2], v[3])};
            *(LAS v2u*)(LP(oVW)) = vv;
        }
        unsigned oVX = (unsigned)(size_t)(L + C_VX + (4 * g + (c16 >> 2)) * RST + (c16 & 3) * 8 + 64 * w);
        unsigned oVM = (unsigned)(size_t)(L + C_VS + c16 * 16 + (g & 1) * 8 + 512 * w);
        unsigned oOW = (unsigned)(size_t)(L + C_MEM + (c16 & 7) * RST + (32 * w + 4 * g) * 2);
        unsigned oOR = (unsigned)(size_t)(L + C_MEM + (c16 & 7) * RST + (32 * (c16 >> 3) + 8 * g) * 2);
        PIN(oVX); PIN(oVM); PIN(oOW); PIN(oOR);
        f32x4 oo[8];
        __syncthreads();
        if (s == NSEG - 1) break;
        bf16x8 aO[2];
        {
            bf16x8 qb[4], km_[4];
#pragma unroll
            for (int kq = 0; kq < 4; ++kq) {
                qb[kq] = *(const LAS bf16x8*)(LP(oQK) + 64 * kq);
                km_[kq] = *(const LAS bf16x8*)(LP(oQK) + (C_KS - C_QS) + 64 * kq);
            }
            bf16x8 va[2]; v2u vm[2];
#pragma unroll
            for (int T = 0; T < 2; ++T) {
                va[T] = cat8(tr_read(LP(oVX) + 32 * T), tr_read(LP(oVX) + 32 * T + 16 * RST));
                vm[T] = *(const LAS v2u*)(LP(oVM) + 256 * T);
            }
            SBAR();
            f32x4 sc[3];
#pragma unroll
            for (int T = 0; T < 3; ++T) sc[T] = (f32x4){0.f, 0.f, 0.f, 0.f};
#pragma unroll
            for (int kq = 0; kq < 4; ++kq) {
                sc[0] = MFMA32(kx[0][kq], qb[kq], sc[0]); sc[1] = MFMA32(kx[1][kq], qb[kq], sc[1]); sc[2] = MFMA32(km_[kq], qb[kq], sc[2]);
            }
            SBAR();
            const float cs = 0.08838834764831845f * 1.4426950408889634f;
            if (g >= 2) sc[2] = (f32x4){-3.0e38f, -3.0e38f, -3.0e38f, -3.0e38f};
            float mx = fmaxf(fmaxf(fmaxf(sc[0][0], sc[0][1]), fmaxf(sc[0][2], sc[0][3])), fmaxf(fmaxf(sc[1][0], sc[1][1]), fmaxf(sc[1][2], sc[1][3])));
            mx = fmaxf(mx, fmaxf(fmaxf(sc[2][0], sc[2][1]), fmaxf(sc[2][2], sc[2][3])));
            mx = xl32_max(xl16_max(mx));
            const float nm = -mx * cs;
            float p[12];
#pragma unroll
            for (int T = 0; T < 3; ++T)
#pragma unroll
                for (int r = 0; r < 4; ++r) p[T * 4 + r] = __builtin_amdgcn_exp2f(__builtin_fmaf(sc[T][r], cs, nm));
            float sum = ((p[0] + p[1]) + (p[2] + p[3])) + ((p[4] + p[5]) + (p[6] + p[7])) + ((p[8] + p[9]) + (p[10] + p[11]));
            sum = xl32_add(xl16_add(sum));
            const float inv = __builtin_amdgcn_rcpf(sum);
            v4u px = {pk2(p[0] * inv, p[1] * inv), pk2(p[2] * inv, p[3] * inv), pk2(p[4] * inv, p[5] * inv), pk2(p[6] * inv, p[7] * inv)};
            const bf16x8 Px = __builtin_bit_cast(bf16x8, px);
            const bf16x8 Pm = __builtin_bit_cast(bf16x8, (v4u){pk2(p[8] * inv, p[9] * inv), pk2(p[10] * inv, p[11] * inv), 0u, 0u});
            f32x4 o[2];
#pragma unroll
            for (int T = 0; T < 2; ++T) o[T] = MFMA32(va[T], Px, ((f32x4){0.f, 0.f, 0.f, 0.f}));
#pragma unroll
            for (int T = 0; T < 2; ++T) o[T] = MFMA32(__builtin_bit_cast(bf16x8, (v4u){vm[T][0], vm[T][1], 0u, 0u}), Pm, o[T]);
            if (c16 < 8) {
#pragma unroll
                for (int T = 0; T < 2; ++T) *(LAS v2u*)(LP(oOW) + 32 * T) = (v2u){pk2(o[T][0], o[T][1]), pk2(o[T][2], o[T][3])};
            }
#pragma unroll
            for (int tt = 0; tt < 8; ++tt) { oo[tt] = (f32x4){0.f, 0.f, 0.f, 0.f}; PIN(oo[tt]); }
            __syncthreads();
            aO[0] = *(const LAS bf16x8*)(LP(oOR));
            aO[1] = *(const LAS bf16x8*)(LP(oOR) + 128);
        }
        const unsigned slot = s & 1, par = (s >> 1) & 1;
        {
#pragma unroll
            for (int tt = 0; tt < 8; ++tt) oo[tt] = SMFMAC(aO[0], Bo[tt][0], oo[tt], sidx);
#pragma unroll
            for (int tt = 0; tt < 8; ++tt) oo[tt] = SMFMAC(aO[1], Bo[tt][1], oo[tt], sidx);
#pragma unroll
            for (int tp = 0; tp < 4; ++tp) {
                const f32x4 o = comb2(oo[2 * tp], oo[2 * tp + 1]);
                v4u st;
#pragma unroll
                for (int r = 0; r < 4; ++r) st[r] = (__float_as_uint(o[r]) & ~1u) | par;
                const unsigned cons = 8 * w + 2 * tp + (l >> 5);
                st16_sc1(st, rRS, slot * 524288u + cons * 16384u + h * 512u + (l & 31) * 16u);
            }
        }
        {
            const int u = t & 127;
            const v4u pub = *(const LAS v4u*)(L + (t < 128 ? C_KS + (u >> 4) * RST + (u & 15) * 16 : C_VS + u * 16));
            const unsigned off = (unsigned)((s * NH + h) * (MM * DH) + u * 8) * 2u;
            if (t < 128) st16_sc1(pub, rKM, off); else st16_sc1(pub, rVM, off);
        }
        if (((s + 1) & 3) == 0) wait_flag_kv(flags + FL_QKV + (((s + 1) >> 2) * 32 + h) * 16);
        v4u pk_[2], pv_[2];
#pragma unroll
        for (int i = 0; i < 2; ++i) {
            const int u = i * 256 + t, r = u >> 4, ch = u & 15;
            pk_[i] = ld16_sc1(rK, (unsigned)(((s + 1) * SEG + r) * HD + h * DH + ch * 8) * 2u);
            pv_[i] = ld16_sc1(rV, (unsigned)(((s + 1) * SEG + r) * HD + h * DH + ch * 8) * 2u);
        }
        const int lr = (l >> 3) & 3;
        unsigned offAG;
        {
            const int m = 4 * (w >> 1) + lr, cp = 4 * (w & 1) + ((l & 7) >> 1);
            offAG = slot * 8192u + (h >> 2) * 1024u + ((h & 3) * 16 + (cp & 1) * 8 + m) * 16u + (cp >> 1) * 4u;
            PIN(offAG);
        }
        dly(DLY1);
        {
            v4u x[4];
            const unsigned base = slot * 524288u + h * 16384u + (l >> 3) * 512u + (8 * w + (l & 7)) * 16u;
            unsigned spins = 0;
            for (;;) {
                asm volatile("" ::: "memory");
                unsigned bad = 0;
#pragma unroll
                for (int i = 0; i < 4; ++i) x[i] = ld16_sc1(rRS, base + i * 4096u);
#pragma unroll
                for (int i = 0; i < 4; ++i) bad |= (x[i][0] ^ par) | (x[i][1] ^ par) | (x[i][2] ^ par) | (x[i][3] ^ par);
                if (__all((bad & 1u) == 0)) break;
                if (++spins > SPIN_CAP) break;
            }
            float sm[4];
#pragma unroll
            for (int r = 0; r < 4; ++r) {
                float v = (__uint_as_float(x[0][r]) + __uint_as_float(x[1][r])) + (__uint_as_float(x[2][r]) + __uint_as_float(x[3][r]));
                sm[r] = xl32_add(xl16_add(xl8_add(v)));
            }
            const float own = lr == 0 ? sm[0] : (lr == 1 ? sm[1] : (lr == 2 ? sm[2] : sm[3]));
            const float nb = xl1_get(own);
            unsigned pkd = pk2(own, nb);
            if (pkd == SENT) pkd = 0x7FC07FC0u;
            if (l < 32 && !(l & 1)) {
                st4_sc1(pkd, rAG, offAG);
                st4_sc1(SENT, rAG, offAG ^ 8192u);
            }
        }
#pragma unroll
        for (int i = 0; i < 2; ++i) {
            const int u = i * 256 + t, r = u >> 4, ch = u & 15;
            *(LAS v4u*)(L + C_KX + r * RST + ch * 16) = pk_[i];
            *(LAS v4u*)(L + C_VX + r * RST + ch * 16) = pv_[i];
        }
    }
    {
        const int u = t & 127;
        const v4u pub = *(const LAS v4u*)(L + (t < 128 ? C_KS + (u >> 4) * RST + (u & 15) * 16 : C_VS + u * 16));
        const unsigned off = (unsigned)(((NSEG - 1) * NH + h) * (MM * DH) + u * 8) * 2u;
        if (t < 128) st16_sc1(pub, rKM, off); else st16_sc1(pub, rVM, off);
        asm volatile("s_waitcnt vmcnt(0)" ::: "memory");
        __syncthreads();
        if (t == 0) __hip_atomic_fetch_add(flags + FL_PROG, 1u, __ATOMIC_RELAXED, __HIP_MEMORY_SCOPE_AGENT);
    }
}

__device__ __forceinline__ void xattn_task(unsigned char* smem, const u16* Q, const u16* Kx, const u16* Vx,
                                           const u16* kmem, const u16* vmem, u16* __restrict__ O, int s, int h, const unsigned* prog) {
    const int t = threadIdx.x, w = t >> 6, l = t & 63, g = l >> 4, c16 = l & 15;
    LAS unsigned char* LV = (LAS unsigned char*)smem + w * 32 * 288;
    LAS unsigned char* LO = (LAS unsigned char*)smem + 4 * 32 * 288 + w * 32 * 272;
    const size_t rowb = (size_t)s * SEG;
#pragma unroll
    for (int i = 0; i < 8; ++i) {
        const int u = i * 64 + l, r = u >> 4, ch = u & 15;
        *(LAS v4u*)(LV + r * 288 + ch * 16) = *(const v4u*)(Vx + (rowb + r) * HD + h * DH + ch * 8);
    }
    f32x4 sc[2][3];
#pragma unroll
    for (int qt = 0; qt < 2; ++qt)
#pragma unroll
        for (int T = 0; T < 3; ++T) sc[qt][T] = (f32x4){0.f, 0.f, 0.f, 0.f};
    bf16x8 q0[4], q1[4];
#pragma unroll
    for (int kq = 0; kq < 4; ++kq) {
        const int eo = 32 * kq + 8 * g;
        q0[kq] = *(const bf16x8*)(Q + (rowb + c16) * HD + h * DH + eo);
        q1[kq] = *(const bf16x8*)(Q + (rowb + 16 + c16) * HD + h * DH + eo);
        const bf16x8 k0 = *(const bf16x8*)(Kx + (rowb + c16) * HD + h * DH + eo);
        const bf16x8 k1 = *(const bf16x8*)(Kx + (rowb + 16 + c16) * HD + h * DH + eo);
        sc[0][0] = MFMA32(k0, q0[kq], sc[0][0]); sc[0][1] = MFMA32(k1, q0[kq], sc[0][1]);
        sc[1][0] = MFMA32(k0, q1[kq], sc[1][0]); sc[1][1] = MFMA32(k1, q1[kq], sc[1][1]);
    }
    if (w == 0) {
        unsigned spins = 0;
        const int nap = s >= NSEG - 2 ? 4 : 40;
        while ((unsigned)__builtin_amdgcn_readfirstlane(ld_flag(prog)) < (unsigned)(s + 1) * NH) { dly(nap); if (++spins > SPIN_CAP) break; }
    }
    __syncthreads();
    const auto rKM = mk_rsrc(kmem, (unsigned)NSEG * NH * MM * DH * 2);
    const auto rVM = mk_rsrc(vmem, (unsigned)NSEG * NH * MM * DH * 2);
    v2u vmf[8];
#pragma unroll
    for (int T = 0; T < 8; ++T) vmf[T] = __builtin_amdgcn_raw_buffer_load_b64(rVM, (unsigned)((s * NH + h) * (MM * DH) + (16 * T + c16) * 8 + (g & 1) * 4) * 2u, 0, 16);
#pragma unroll
    for (int kq = 0; kq < 4; ++kq) {
        const bf16x8 k2 = __builtin_bit_cast(bf16x8, ld16_sc1(rKM, (unsigned)(((s * NH + h) * MM + (c16 & 7)) * DH + 32 * kq + 8 * g) * 2u));
        sc[0][2] = MFMA32(k2, q0[kq], sc[0][2]); sc[1][2] = MFMA32(k2, q1[kq], sc[1][2]);
    }
    bf16x8 Px[2], Pm[2];
    const float scale = 0.08838834764831845f;
#pragma unroll
    for (int qt = 0; qt < 2; ++qt) {
        const int qi = 16 * qt + c16;
        float p[12]; float mx = -3.0e38f;
#pragma unroll
        for (int T = 0; T < 3; ++T)
#pragma unroll
            for (int r = 0; r < 4; ++r) {
                float v = rbf(rbf(sc[qt][T][r]) * scale);
                const bool valid = (T == 2) ? (g < 2) : (16 * T + 4 * g + r <= qi);
                if (!valid) v = -3.0e38f;
                p[T * 4 + r] = v; mx = fmaxf(mx, v);
            }
        mx = fmaxf(mx, __shfl_xor(mx, 16)); mx = fmaxf(mx, __shfl_xor(mx, 32));
        float sum = 0.f;
#pragma unroll
        for (int i = 0; i < 12; ++i) { p[i] = (p[i] < -1.0e38f) ? 0.f : __expf(p[i] - mx); sum += p[i]; }
        sum += __shfl_xor(sum, 16); sum += __shfl_xor(sum, 32);
        const float inv = 1.0f / sum;
        v4u px = {pk2(p[0] * inv, p[1] * inv), pk2(p[2] * inv, p[3] * inv), pk2(p[4] * inv, p[5] * inv), pk2(p[6] * inv, p[7] * inv)};
        v2u pm = {pk2(p[8] * inv, p[9] * inv), pk2(p[10] * inv, p[11] * inv)};
        Px[qt] = __builtin_bit_cast(bf16x8, px); Pm[qt] = __builtin_bit_cast(bf16x8, (v4u){pm[0], pm[1], 0u, 0u});
    }
#pragma unroll
    for (int T = 0; T < 8; ++T) {
        const LAS unsigned char* pv = LV + (4 * g + (c16 >> 2)) * 288 + (16 * T + (c16 & 3) * 4) * 2;
        const bf16x8 va = cat8(tr_read(pv), tr_read(pv + 16 * 288));
        v2u vm = vmf[T];
        if (g >= 2) vm = (v2u){0u, 0u};
#pragma unroll
        for (int qt = 0; qt < 2; ++qt) {
            f32x4 o = (f32x4){0.f, 0.f, 0.f, 0.f};
            o = MFMA32(va, Px[qt], o);
            o = MFMA32(__builtin_bit_cast(bf16x8, (v4u){vm[0], vm[1], 0u, 0u}), Pm[qt], o);
            v2u st = {pk2(o[0], o[1]), pk2(o[2], o[3])};
            *(LAS v2u*)(LO + (16 * qt + c16) * 272 + (16 * T + 4 * g) * 2) = st;
        }
    }
    const auto rO = mk_rsrc(O, (unsigned)S_LEN * HD * 2);
#pragma unroll
    for (int i = 0; i < 8; ++i) {
        const int u = i * 64 + l, r = u >> 4, ch = u & 15;
        st16_sc1(*(const LAS v4u*)(LO + r * 272 + ch * 16), rO, (unsigned)((rowb + r) * HD + h * DH + ch * 8) * 2u);
    }
}

constexpr int NBULK = 224;
#define NRB_IN 12
struct Params {
    const u16 *x, *mem0, *Wq, *Wk, *Wv, *Wo, *wg, *wu, *wd;
    u16* out; unsigned char* ws;
};
__device__ __forceinline__ void bulk_wait(const unsigned* p, unsigned want) {
    if (threadIdx.x < 64) {
        unsigned spins = 0;
        while ((unsigned)__builtin_amdgcn_readfirstlane(ld_flag(p)) < want) { __builtin_amdgcn_s_sleep(48); if (++spins > SPIN_CAP) break; }
    }
    __syncthreads();
    __builtin_amdgcn_fence(__ATOMIC_ACQUIRE, "agent");
    asm volatile("s_waitcnt vmcnt(0)" ::: "memory");
}
__device__ __forceinline__ void bulk_arrive(unsigned* p) {
    asm volatile("s_waitcnt vmcnt(0)" ::: "memory");
    __syncthreads();
    if (threadIdx.x == 0) __hip_atomic_fetch_add(p, 1u, __ATOMIC_RELAXED, __HIP_MEMORY_SCOPE_AGENT);
}
__global__ void __launch_bounds__(256, 1) k_fused(Params P) {
    extern __shared__ __attribute__((aligned(16))) unsigned char smem[];
    unsigned char* ws = P.ws;
    u16* xn = (u16*)(ws + WS_XN); u16* Q = (u16*)(ws + WS_Q); u16* Kx = (u16*)(ws + WS_K); u16* Vx = (u16*)(ws + WS_V);
    u16* O = (u16*)(ws + WS_O); u16* kmem = (u16*)(ws + WS_KM); u16* vmem = (u16*)(ws + WS_VM);
    if (blockIdx.x < NH) {
        chain_body(smem, P.Wq, P.Wk, P.Wv, P.Wo, P.mem0, Kx, Vx, kmem, vmem, ws);
        return;
    }
    u16* H = (u16*)(ws + WS_H); u16* HN = (u16*)(ws + WS_HN); u16* ACT = (u16*)(ws + WS_ACT);
    unsigned* flags = (unsigned*)(ws + WS_FLAGS);
    const int b = blockIdx.x - NH, t = threadIdx.x, w = __builtin_amdgcn_readfirstlane(t >> 6), l = t & 63;
    for (int i = b; i < 16 * 96; i += NBULK) {
        int rb, hh, mat;
        if (i < 1024) { rb = i >> 6; hh = (i >> 1) & 31; mat = 1 + (i & 1); }
        else { const int j = i - 1024; rb = j >> 5; hh = j & 31; mat = 0; }
        const u16* W = mat == 0 ? P.Wq : (mat == 1 ? P.Wk : P.Wv);
        u16* C = mat == 0 ? Q : (mat == 1 ? Kx : Vx);
        gemm_tile<0, true, 128, 128>(smem, xn, DM, W, nullptr, HD, C, HD, nullptr, DM, rb * 128, hh * DH);
        asm volatile("s_waitcnt vmcnt(0)" ::: "memory");
        __syncthreads();
        if (t == 0) __hip_atomic_fetch_add(flags + FL_QKV + (rb * 32 + hh) * 16, mat == 0 ? 4u : 1u, __ATOMIC_RELAXED, __HIP_MEMORY_SCOPE_AGENT);
    }
    float* slab = (float*)(ws + WS_SLAB);
    const int role = b < 48 ? 0 : (b < 112 ? 1 : (b < 128 ? 2 : (b < 192 ? 3 : 4)));
    const int rb0 = role == 0 ? 0 : (role == 1 ? 48 : (role == 2 ? 112 : (role == 3 ? 128 : 192)));
    const int nrole = role == 0 ? 48 : (role == 1 ? 64 : (role == 2 ? 16 : (role == 3 ? 64 : 32)));
    const int ntask = role == 0 ? 32 : (role == 1 ? 16 : (role == 2 ? 16 : (role == 3 ? 28 : 8)));
    const int nrb = role == 0 ? 16 : NRB_IN;
    for (int idx = b - rb0; idx < nrb * ntask; idx += nrole) {
        const int rb = idx / ntask, r = idx % ntask, m0 = rb * 128;
        unsigned* gf = flags + FL_GRP + rb * 4 * 16;
        if (role == 0) {
            const int s = 4 * rb + (r >> 3), hh = (r & 7) * 4 + w;
            wait_flag_ge(flags + FL_QKV + (rb * 32 + hh) * 16, 6, 32);
            __builtin_amdgcn_fence(__ATOMIC_ACQUIRE, "agent");
            asm volatile("s_waitcnt vmcnt(0)" ::: "memory");
            __syncthreads();
            xattn_task(smem, Q, Kx, Vx, kmem, vmem, O, s, hh, flags + FL_PROG);
            bulk_arrive(gf + 0 * 16);
        } else if (role == 1) {
            const int ks = r >> 2, nt = r & 3;
            bulk_wait(gf + 0 * 16, 32);
            gemm_tile<4, true, 128, 128>(smem, O + ks * 1024, HD, P.Wo + (size_t)ks * 1024 * DM, nullptr, DM, (u16*)(slab + (size_t)ks * S_LEN * DM), DM, nullptr, 1024, m0, nt * 128);
            bulk_arrive(gf + 1 * 16);
        } else if (role == 2) {
            bulk_wait(gf + 1 * 16, 16);
#pragma unroll
            for (int rr = 0; rr < 2; ++rr) {
                const int row = m0 + r * 8 + w * 2 + rr;
                f32x4 a0 = (f32x4){0.f, 0.f, 0.f, 0.f}, a1 = a0;
#pragma unroll
                for (int ks = 0; ks < 4; ++ks) {
                    const float* src = slab + ((size_t)ks * S_LEN + row) * DM + l * 8;
                    a0 += *(const f32x4*)src; a1 += *(const f32x4*)(src + 4);
                }
                const v4u xr = *(const v4u*)(P.x + (size_t)row * DM + l * 8);
                float v[8];
#pragma unroll
                for (int i = 0; i < 4; ++i) {
                    const float lo = i < 2 ? a0[2 * i] : a1[2 * i - 4], hi = i < 2 ? a0[2 * i + 1] : a1[2 * i - 3];
                    v[2 * i] = rbf(__uint_as_float(xr[i] << 16) + rbf(lo)); v[2 * i + 1] = rbf(__uint_as_float(xr[i] & 0xffff0000u) + rbf(hi));
                }
                float sm = 0.f;
#pragma unroll
                for (int i = 0; i < 8; ++i) sm += v[i];
#pragma unroll
                for (int o = 32; o >= 1; o >>= 1) sm += __shfl_xor(sm, o);
                const float mean = sm * (1.0f / DM);
                float q = 0.f;
#pragma unroll
                for (int i = 0; i < 8; ++i) { const float d = v[i] - mean; q += d * d; }
#pragma unroll
                for (int o = 32; o >= 1; o >>= 1) q += __shfl_xor(q, o);
                const float mb = rbf(mean), den = rbf(sqrtf(rbf(rbf(q * (1.0f / DM)) + 1e-5f)));
                v2u h0 = {pk2(v[0], v[1]), pk2(v[2], v[3])}, h1 = {pk2(v[4], v[5]), pk2(v[6], v[7])};
                v2u n0 = {pk2(rbf(v[0] - mb) / den, rbf(v[1] - mb) / den), pk2(rbf(v[2] - mb) / den, rbf(v[3] - mb) / den)};
                v2u n1 = {pk2(rbf(v[4] - mb) / den, rbf(v[5] - mb) / den), pk2(rbf(v[6] - mb) / den, rbf(v[7] - mb) / den)};
                st8_dev(H + (size_t)row * DM + l * 8, h0); st8_dev(H + (size_t)row * DM + l * 8 + 4, h1);
                st8_dev(HN + (size_t)row * DM + l * 8, n0); st8_dev(HN + (size_t)row * DM + l * 8 + 4, n1);
            }
            bulk_arrive(gf + 2 * 16);
        } else if (role == 3) {
            bulk_wait(gf + 2 * 16, 16);
            gemm_tile<2, true, 128, 128>(smem, HN, DM, P.wg, P.wu, FF, ACT, FF, nullptr, DM, m0, r * 64);
            bulk_arrive(gf + 3 * 16);
        } else {
            bulk_wait(gf + 3 * 16, 28);
            gemm_tile<1, false, 128, 64>(smem, ACT, FF, P.wd, nullptr, DM, P.out, DM, H, FF, m0, r * 64);
        }
    }
}

extern "C" void kernel_launch(void* const* d_in, const int* in_sizes, int n_in, void* d_out, int out_size, void* d_ws, size_t ws_size,
                              hipStream_t stream) {
    const u16* x = (const u16*)d_in[0];
    const u16* mem0 = (const u16*)d_in[1];
    const u16* Wq = (const u16*)d_in[2];
    const u16* Wk = (const u16*)d_in[3];
    const u16* Wv = (const u16*)d_in[4];
    const u16* Wo = (const u16*)d_in[5];
    const u16* wg = (const u16*)d_in[8];
    const u16* wu = (const u16*)d_in[9];
    const u16* wd = (const u16*)d_in[10];
    u16* out = (u16*)d_out;
    unsigned char* ws = (unsigned char*)d_ws;
    u16* xn = (u16*)(ws + WS_XN); u16* Q = (u16*)(ws + WS_Q); u16* K = (u16*)(ws + WS_K); u16* V = (u16*)(ws + WS_V);
    u16* O = (u16*)(ws + WS_O); u16* km = (u16*)(ws + WS_KM); u16* vm = (u16*)(ws + WS_VM);
    u16* H = (u16*)(ws + WS_H); u16* HN = (u16*)(ws + WS_HN); u16* ACT = (u16*)(ws + WS_ACT);

    (void)hipFuncSetAttribute((const void*)k_fused, hipFuncAttributeMaxDynamicSharedMemorySize, C_LDS);
#define TBM 64
#define TBN 64
#define TBM1 64
    (void)hipFuncSetAttribute((const void*)k_gemm<2, TBM1, 128>, hipFuncAttributeMaxDynamicSharedMemorySize, 2 * g_stage(TBM1, 128));
    (void)hipFuncSetAttribute((const void*)k_gemm_splitk<TBM, TBN>, hipFuncAttributeMaxDynamicSharedMemorySize, 2 * g_stage(TBM, TBN));
    k_ln<true><<<S_LEN / 4, 256, 0, stream>>>(x, xn, ws);
    Params P{x, mem0, Wq, Wk, Wv, Wo, wg, wu, wd, out, ws};
    k_fused<<<NH + NBULK, 256, C_LDS, stream>>>(P);
    float* slab = (float*)(ws + WS_SLAB);
    constexpr int R0 = NRB_IN * 128, NR = S_LEN - R0;
    k_gemm_splitk<TBM, TBN><<<dim3(DM / TBN, NR / TBM, 8), 256, 2 * g_stage(TBM, TBN), stream>>>(O, HD, Wo, DM, slab, HD / 8, R0);
    k_reduce<8, true><<<NR / 4, 256, 0, stream>>>(slab, x, H, HN, R0);
    k_gemm<2, TBM1, 128><<<dim3(FF / 64, NR / TBM1), 256, 2 * g_stage(TBM1, 128), stream>>>(HN, DM, wg, wu, FF, ACT, FF, nullptr, DM, R0);
    k_gemm_splitk<TBM, TBN><<<dim3(DM / TBN, NR / TBM, 7), 256, 2 * g_stage(TBM, TBN), stream>>>(ACT, FF, wd, DM, slab, FF / 7, R0);
    k_reduce<7, false><<<NR / 4, 256, 0, stream>>>(slab, H, out, nullptr, R0);
}
```

```cpp
#include <hip/hip_runtime.h>
#include <hip/hip_bf16.h>
#include <stdint.h>

typedef unsigned short u16;
typedef __bf16 bf16x8 __attribute__((ext_vector_type(8)));
typedef __bf16 bf16x4 __attribute__((ext_vector_type(4)));
typedef __bf16 bf16x16 __attribute__((ext_vector_type(16)));
typedef short s16x4 __attribute__((ext_vector_type(4)));
typedef float f32x4 __attribute__((ext_vector_type(4)));
typedef unsigned v4u __attribute__((ext_vector_type(4)));
typedef unsigned v2u __attribute__((ext_vector_type(2)));
#define LAS __attribute__((address_space(3)))

constexpr int S_LEN = 2048, DM = 512, NH = 32, DH = 128, HD = NH * DH, MM = 8, SEG = 32, NSEG = S_LEN / SEG, FF = 1792;

constexpr size_t WS_RS = 0;
constexpr size_t WS_AG = WS_RS + 2u * 32 * 32 * 512;
constexpr size_t WS_SYNC_END = WS_AG + 2u * 8192;
constexpr size_t WS_FLAGS = WS_SYNC_END;
constexpr int FL_QKV = 0;
constexpr int FL_PROG = 16 * 32 * 16;
constexpr int FL_GRP = FL_PROG + 64;
constexpr int FL_WORDS = FL_GRP + 16 * 4 * 16;
constexpr size_t WS_FLAGS_END = WS_FLAGS + FL_WORDS * 4;
constexpr size_t WS_XN = 2u << 20;
constexpr size_t WS_Q = WS_XN + (size_t)S_LEN * DM * 2;
constexpr size_t WS_K = WS_Q + (size_t)S_LEN * HD * 2;
constexpr size_t WS_V = WS_K + (size_t)S_LEN * HD * 2;
constexpr size_t WS_O = WS_V + (size_t)S_LEN * HD * 2;
constexpr size_t WS_KM = WS_O + (size_t)S_LEN * HD * 2;
constexpr size_t WS_VM = WS_KM + (size_t)NSEG * NH * MM * DH * 2;
constexpr size_t WS_H = WS_VM + (size_t)NSEG * NH * MM * DH * 2;
constexpr size_t WS_HN = WS_H + (size_t)S_LEN * DM * 2;
constexpr size_t WS_ACT = WS_HN + (size_t)S_LEN * DM * 2;
constexpr size_t WS_SLAB = WS_ACT + (size_t)S_LEN * FF * 2;
constexpr size_t WS_END = WS_SLAB + (size_t)8 * S_LEN * DM * 4;

__device__ __forceinline__ float bf2f(u16 v) { return __uint_as_float((unsigned)v << 16); }
__device__ __forceinline__ u16 f2bf(float f) { return __builtin_bit_cast(u16, (__bf16)f); }
__device__ __forceinline__ float rbf(float f) { return (float)(__bf16)f; }
typedef __bf16 bf16x2 __attribute__((ext_vector_type(2)));
typedef float f32x2 __attribute__((ext_vector_type(2)));
__device__ __forceinline__ unsigned pk2(float lo, float hi) {
    const f32x2 v = {lo, hi};
    return __builtin_bit_cast(unsigned, __builtin_convertvector(v, bf16x2));
}
__device__ __forceinline__ float xl32_add(float v) { auto r = __builtin_amdgcn_permlane32_swap(__float_as_uint(v), __float_as_uint(v), false, false); return __uint_as_float(r[0]) + __uint_as_float(r[1]); }
__device__ __forceinline__ float xl16_add(float v) { auto r = __builtin_amdgcn_permlane16_swap(__float_as_uint(v), __float_as_uint(v), false, false); return __uint_as_float(r[0]) + __uint_as_float(r[1]); }
__device__ __forceinline__ float xl32_max(float v) { auto r = __builtin_amdgcn_permlane32_swap(__float_as_uint(v), __float_as_uint(v), false, false); return fmaxf(__uint_as_float(r[0]), __uint_as_float(r[1])); }
__device__ __forceinline__ float xl16_max(float v) { auto r = __builtin_amdgcn_permlane16_swap(__float_as_uint(v), __float_as_uint(v), false, false); return fmaxf(__uint_as_float(r[0]), __uint_as_float(r[1])); }
__device__ __forceinline__ float xl8_add(float v) { return v + __uint_as_float(__builtin_amdgcn_update_dpp(0, __float_as_uint(v), 0x128, 0xf, 0xf, false)); }
__device__ __forceinline__ float xl1_get(float v) { return __uint_as_float(__builtin_amdgcn_update_dpp(0, __float_as_uint(v), 0xB1, 0xf, 0xf, false)); }
#define SBAR() __builtin_amdgcn_sched_barrier(0)
__device__ __forceinline__ s16x4 tr_read(const LAS unsigned char* p) {
    return __builtin_amdgcn_ds_read_tr16_b64_v4i16((LAS s16x4*)p);
}
__device__ __forceinline__ bf16x8 cat8(s16x4 a, s16x4 b) {
    typedef short s16x8 __attribute__((ext_vector_type(8)));
    s16x8 r = __builtin_shufflevector(a, b, 0, 1, 2, 3, 4, 5, 6, 7);
    return __builtin_bit_cast(bf16x8, r);
}
__device__ __forceinline__ bf16x16 cat16(bf16x8 a, bf16x8 b) {
    return __builtin_shufflevector(a, b, 0, 1, 2, 3, 4, 5, 6, 7, 8, 9, 10, 11, 12, 13, 14, 15);
}
#define MFMA32(a, b, c) __builtin_amdgcn_mfma_f32_16x16x32_bf16((a), (b), (c), 0, 0, 0)
#define SMFMAC(a, b, c, idx) __builtin_amdgcn_smfmac_f32_16x16x64_bf16((a), (b), (c), (idx), 0, 0)

template <bool INIT>
__global__ void __launch_bounds__(256) k_ln(const u16* __restrict__ x, u16* __restrict__ y, unsigned char* __restrict__ ws) {
    const int row = blockIdx.x * 4 + (threadIdx.x >> 6), lane = threadIdx.x & 63;
    if (INIT) {
        const size_t u = (size_t)blockIdx.x * 256 + threadIdx.x;
        if (u * 16 < WS_SYNC_END) *(v4u*)(ws + u * 16) = (v4u){0xFFFFFFFFu, 0xFFFFFFFFu, 0xFFFFFFFFu, 0xFFFFFFFFu};
        else if (u * 16 < WS_FLAGS_END) *(v4u*)(ws + u * 16) = (v4u){0u, 0u, 0u, 0u};
    }
    const v4u raw = *(const v4u*)(x + (size_t)row * DM + lane * 8);
    float v[8];
#pragma unroll
    for (int i = 0; i < 4; ++i) { v[2 * i] = __uint_as_float(raw[i] << 16); v[2 * i + 1] = __uint_as_float(raw[i] & 0xffff0000u); }
    float s = 0.f;
#pragma unroll
    for (int i = 0; i < 8; ++i) s += v[i];
#pragma unroll
    for (int o = 32; o >= 1; o >>= 1) s += __shfl_xor(s, o);
    const float mean = s * (1.0f / DM);
    float q = 0.f;
#pragma unroll
    for (int i = 0; i < 8; ++i) { const float d = v[i] - mean; q += d * d; }
#pragma unroll
    for (int o = 32; o >= 1; o >>= 1) q += __shfl_xor(q, o);
    const float mb = rbf(mean), vb = rbf(q * (1.0f / DM));
    const float den = rbf(sqrtf(rbf(vb + 1e-5f)));
    v4u o;
#pragma unroll
    for (int i = 0; i < 4; ++i) o[i] = pk2(rbf(v[2 * i] - mb) / den, rbf(v[2 * i + 1] - mb) / den);
    *(v4u*)(y + (size_t)row * DM + lane * 8) = o;
}

constexpr int G_BK = 64, G_AST = 144;
__host__ __device__ constexpr int g_stage(int BM, int BN) { return BM * G_AST + G_BK * (BN * 2 + 32); }
__device__ __forceinline__ void st8_dev(u16* p, v2u v) {
    __hip_atomic_store((unsigned long long*)p, (unsigned long long)v[0] | ((unsigned long long)v[1] << 32), __ATOMIC_RELAXED, __HIP_MEMORY_SCOPE_AGENT);
}
template <int EPI, bool DEV, int BM, int BN>
__device__ __forceinline__ void gemm_tile(unsigned char* smem, const u16* __restrict__ A, int lda, const u16* __restrict__ B, const u16* __restrict__ B2, int ldb,
                                          u16* __restrict__ C, int ldc, const u16* __restrict__ res, int K, int m0, int n0) {
    constexpr int BST = BN * 2 + 32, ABYTES = BM * G_AST, STAGE = g_stage(BM, BN);
    constexpr int NA = BM / 32, NB = BN / 32, MI = BM / 32, NJ = BN / 32, BCH = BN / 8;
    const int t = threadIdx.x, w = t >> 6, l = t & 63, g = l >> 4, c16 = l & 15;
    const int wr = w >> 1, wc = w & 1;
    v4u ra[4][NA], rb[4][NB];
    auto gload = [&](int k0, v4u (&da)[NA], v4u (&db)[NB]) {
#pragma unroll
        for (int i = 0; i < NA; ++i) { const int u = i * 256 + t; da[i] = *(const v4u*)(A + (size_t)(m0 + (u >> 3)) * lda + k0 + (u & 7) * 8); }
#pragma unroll
        for (int i = 0; i < NB; ++i) {
            const int u = i * 256 + t, kr = u / BCH, ch = u % BCH;
            if (EPI == 2) {
                constexpr int Q = BCH / 4;
                const int wcc = ch / (2 * Q), part = (ch / Q) & 1, cc = (ch % Q) * 8;
                db[i] = *(const v4u*)((part ? B2 : B) + (size_t)(k0 + kr) * ldb + n0 + (BN / 4) * wcc + cc);
            } else db[i] = *(const v4u*)(B + (size_t)(k0 + kr) * ldb + n0 + ch * 8);
        }
    };
    auto swrite = [&](int buf, v4u (&da)[NA], v4u (&db)[NB]) {
        unsigned char* sa = smem + buf * STAGE; unsigned char* sb = sa + ABYTES;
#pragma unroll
        for (int i = 0; i < NA; ++i) { const int u = i * 256 + t; *(v4u*)(sa + (u >> 3) * G_AST + (u & 7) * 16) = da[i]; }
#pragma unroll
        for (int i = 0; i < NB; ++i) { const int u = i * 256 + t; *(v4u*)(sb + (u / BCH) * BST + (u % BCH) * 16) = db[i]; }
    };
    f32x4 acc[MI][NJ];
#pragma unroll
    for (int i = 0; i < MI; ++i)
#pragma unroll
        for (int j = 0; j < NJ; ++j) acc[i][j] = (f32x4){0.f, 0.f, 0.f, 0.f};
    bf16x8 af[2][MI], bfr[2][NJ];
    auto fread = [&](int buf) {
        const LAS unsigned char* sa = (const LAS unsigned char*)smem + buf * STAGE;
        const LAS unsigned char* sb = sa + ABYTES;
#pragma unroll
        for (int ks = 0; ks < 2; ++ks) {
#pragma unroll
            for (int i = 0; i < MI; ++i) af[ks][i] = *(const LAS bf16x8*)(sa + (wr * (BM / 2) + i * 16 + c16) * G_AST + (ks * 32 + g * 8) * 2);
#pragma unroll
            for (int j = 0; j < NJ; ++j) {
                const LAS unsigned char* p = sb + (ks * 32 + g * 8 + (c16 >> 2)) * BST + (wc * (BN / 2) + j * 16 + (c16 & 3) * 4) * 2;
                bfr[ks][j] = cat8(tr_read(p), tr_read(p + 4 * BST));
            }
        }
    };
    auto mma = [&](int ks) {
#pragma unroll
        for (int i = 0; i < MI; ++i)
#pragma unroll
            for (int j = 0; j < NJ; ++j) acc[i][j] = MFMA32(bfr[ks][j], af[ks][i], acc[i][j]);
    };
    const int nk = K / G_BK;
    const int klast = K - G_BK;
#pragma unroll
    for (int i = 0; i < 4; ++i) gload(i * G_BK, ra[i], rb[i]);
    __syncthreads();
    swrite(0, ra[0], rb[0]);
    gload(min(4 * G_BK, klast), ra[0], rb[0]);
    __syncthreads();
    for (int kt = 0; kt < nk; kt += 4) {
#pragma unroll
        for (int u = 0; u < 4; ++u) {
            fread(u & 1);
            mma(0);
            swrite((u + 1) & 1, ra[(u + 1) & 3], rb[(u + 1) & 3]);
            gload(min((kt + u + 5) * G_BK, klast), ra[(u + 1) & 3], rb[(u + 1) & 3]);
            mma(1);
            __syncthreads();
        }
    }
    if (EPI == 4) {
        const auto rC = __builtin_amdgcn_make_buffer_rsrc((void*)C, 0, (unsigned)S_LEN * DM * 4, 0x00020000);
#pragma unroll
        for (int i = 0; i < MI; ++i)
#pragma unroll
            for (int j = 0; j < NJ; ++j)
                __builtin_amdgcn_raw_buffer_store_b128(__builtin_bit_cast(v4u, acc[i][j]), rC, (unsigned)((m0 + wr * (BM / 2) + i * 16 + c16) * ldc + n0 + wc * (BN / 2) + j * 16 + 4 * g) * 4u, 0, DEV ? 16 : 0);
        return;
    }
    constexpr int ON = EPI == 2 ? BN / 2 : BN, OST = ON * 2 + 16;
#pragma unroll
    for (int i = 0; i < MI; ++i) {
        const int m = m0 + wr * (BM / 2) + i * 16 + c16;
        if (EPI == 2) {
#pragma unroll
            for (int j = 0; j < NJ / 2; ++j) {
                float o[4];
#pragma unroll
                for (int r = 0; r < 4; ++r) {
                    const float gt = rbf(acc[i][j][r]), up = rbf(acc[i][j + NJ / 2][r]);
                    const float inner = 0.7978845608028654f * (gt + 0.044715f * gt * gt * gt);
                    const float th = 1.0f - 2.0f * __builtin_amdgcn_rcpf(__expf(2.0f * inner) + 1.0f);
                    const float ge = rbf(0.5f * gt * (1.0f + th));
                    o[r] = ge * up;
                }
                v2u st = {pk2(o[0], o[1]), pk2(o[2], o[3])};
                u16* dst = C + (size_t)m * ldc + n0 + wc * (BN / 4) + j * 16 + 4 * g;
                if (DEV) *(v2u*)(smem + (m - m0) * OST + (wc * (BN / 4) + j * 16 + 4 * g) * 2) = st; else *(v2u*)dst = st;
            }
        } else {
#pragma unroll
            for (int j = 0; j < NJ; ++j) {
                const int n = n0 + wc * (BN / 2) + j * 16 + 4 * g;
                float o[4];
#pragma unroll
                for (int r = 0; r < 4; ++r) o[r] = acc[i][j][r];
                if (EPI == 1) {
                    const v2u rr = *(const v2u*)(res + (size_t)m * ldc + n);
                    o[0] = __uint_as_float(rr[0] << 16) + rbf(o[0]); o[1] = __uint_as_float(rr[0] & 0xffff0000u) + rbf(o[1]);
                    o[2] = __uint_as_float(rr[1] << 16) + rbf(o[2]); o[3] = __uint_as_float(rr[1] & 0xffff0000u) + rbf(o[3]);
                }
                v2u st = {pk2(o[0], o[1]), pk2(o[2], o[3])};
                if (DEV) *(v2u*)(smem + (m - m0) * OST + (n - n0) * 2) = st; else *(v2u*)(C + (size_t)m * ldc + n) = st;
            }
        }
    }
    if (DEV) {
        __syncthreads();
        constexpr int CH = ON / 8;
        const auto rC = __builtin_amdgcn_make_buffer_rsrc((void*)C, 0, 0x7fffffffu, 0x00020000);
#pragma unroll
        for (int i = 0; i < BM * CH / 256; ++i) {
            const int u = i * 256 + t, row = u / CH, ch = u % CH;
            __builtin_amdgcn_raw_buffer_store_b128(*(const v4u*)(smem + row * OST + ch * 16), rC, (unsigned)((m0 + row) * ldc + n0 + ch * 8) * 2u, 0, 16);
        }
    }
}
template <int EPI, int BM, int BN>
__global__ void __launch_bounds__(256) k_gemm(const u16* __restrict__ A, int lda, const u16* __restrict__ B, const u16* __restrict__ B2, int ldb,
                                              u16* __restrict__ C, int ldc, const u16* __restrict__ res, int K, int row0) {
    extern __shared__ __attribute__((aligned(16))) unsigned char smem[];
    gemm_tile<EPI, false, BM, BN>(smem, A, lda, B, B2, ldb, C, ldc, res, K, row0 + blockIdx.y * BM, blockIdx.x * (EPI == 2 ? BN / 2 : BN));
}

template <int BM, int BN>
__global__ void __launch_bounds__(256) k_gemm_splitk(const u16* __restrict__ A, int lda, const u16* __restrict__ B, int ldb, float* __restrict__ slab, int Kpart, int row0) {
    extern __shared__ __attribute__((aligned(16))) unsigned char smem[];
    const int z = blockIdx.z;
    gemm_tile<4, false, BM, BN>(smem, A + (size_t)z * Kpart, lda, B + (size_t)z * Kpart * ldb, nullptr, ldb, (u16*)(slab + (size_t)z * S_LEN * DM), DM, nullptr, Kpart, row0 + blockIdx.y * BM, blockIdx.x * BN);
}
template <int NS, bool LN>
__global__ void __launch_bounds__(256) k_reduce(const float* __restrict__ slab, const u16* __restrict__ res, u16* __restrict__ hout, u16* __restrict__ nout, int row0) {
    const int row = row0 + blockIdx.x * 4 + (threadIdx.x >> 6), l = threadIdx.x & 63;
    f32x4 a0 = (f32x4){0.f, 0.f, 0.f, 0.f}, a1 = a0;
#pragma unroll
    for (int ks = 0; ks < NS; ++ks) {
        const float* src = slab + ((size_t)ks * S_LEN + row) * DM + l * 8;
        a0 += *(const f32x4*)src; a1 += *(const f32x4*)(src + 4);
    }
    const v4u xr = *(const v4u*)(res + (size_t)row * DM + l * 8);
    float v[8];
#pragma unroll
    for (int i = 0; i < 4; ++i) {
        const float lo = i < 2 ? a0[2 * i] : a1[2 * i - 4], hi = i < 2 ? a0[2 * i + 1] : a1[2 * i - 3];
        v[2 * i] = rbf(__uint_as_float(xr[i] << 16) + rbf(lo)); v[2 * i + 1] = rbf(__uint_as_float(xr[i] & 0xffff0000u) + rbf(hi));
    }
    *(v4u*)(hout + (size_t)row * DM + l * 8) = (v4u){pk2(v[0], v[1]), pk2(v[2], v[3]), pk2(v[4], v[5]), pk2(v[6], v[7])};
    if (LN) {
        float sm = 0.f;
#pragma unroll
        for (int i = 0; i < 8; ++i) sm += v[i];
#pragma unroll
        for (int o = 32; o >= 1; o >>= 1) sm += __shfl_xor(sm, o);
        const float mean = sm * (1.0f / DM);
        float q = 0.f;
#pragma unroll
        for (int i = 0; i < 8; ++i) { const float d = v[i] - mean; q += d * d; }
#pragma unroll
        for (int o = 32; o >= 1; o >>= 1) q += __shfl_xor(q, o);
        const float mb = rbf(mean), den = rbf(sqrtf(rbf(rbf(q * (1.0f / DM)) + 1e-5f)));
        v4u o;
#pragma unroll
        for (int i = 0; i < 4; ++i) o[i] = pk2(rbf(v[2 * i] - mb) / den, rbf(v[2 * i + 1] - mb) / den);
        *(v4u*)(nout + (size_t)row * DM + l * 8) = o;
    }
}

constexpr unsigned SENT = 0xFFFFFFFFu;
constexpr int C_WV = 0;
constexpr int C_MEM = 131072;
constexpr int RST = 544;
constexpr int C_QS = C_MEM + 8192;
constexpr int C_KS = C_QS + 256;
constexpr int C_VS = C_QS + 8 * RST;
constexpr int C_KX = C_VS + 2048;
constexpr int C_VX = C_KX + 256;
constexpr int C_LDS = C_KX + 32 * RST;
constexpr unsigned SPIN_CAP = 1u << 22;
#define DLY1 14
#define DLY2 12

__device__ __forceinline__ __amdgpu_buffer_rsrc_t mk_rsrc(const void* p, unsigned bytes) {
    return __builtin_amdgcn_make_buffer_rsrc((void*)p, 0, bytes, 0x00020000);
}
__device__ __forceinline__ v4u ld16_sc1(__amdgpu_buffer_rsrc_t r, unsigned off) { return __builtin_amdgcn_raw_buffer_load_b128(r, off, 0, 16); }
__device__ __forceinline__ void st16_sc1(v4u v, __amdgpu_buffer_rsrc_t r, unsigned off) { __builtin_amdgcn_raw_buffer_store_b128(v, r, off, 0, 16); }
__device__ __forceinline__ void st4_sc1(unsigned v, __amdgpu_buffer_rsrc_t r, unsigned off) { __builtin_amdgcn_raw_buffer_store_b32(v, r, off, 0, 16); }
__device__ __forceinline__ void dly(int n) { for (int i = 0; i < n; ++i) __builtin_amdgcn_s_sleep(1); }

__device__ __forceinline__ f32x4 comb2(f32x4 a, f32x4 b) {
    f32x4 o;
#pragma unroll
    for (int r = 0; r < 4; ++r) {
        auto sw = __builtin_amdgcn_permlane32_swap(__float_as_uint(a[r]), __float_as_uint(b[r]), false, false);
        o[r] = __uint_as_float(sw[0]) + __uint_as_float(sw[1]);
    }
    return o;
}

__device__ __forceinline__ unsigned ld_flag(const unsigned* p) { return __hip_atomic_load(p, __ATOMIC_RELAXED, __HIP_MEMORY_SCOPE_AGENT); }
__device__ __forceinline__ void wait_flag_ge(const unsigned* p, unsigned want, int sleep_n) {
    unsigned spins = 0;
    while ((unsigned)__builtin_amdgcn_readfirstlane(ld_flag(p)) < want) { dly(sleep_n); if (++spins > SPIN_CAP) break; }
    asm volatile("" ::: "memory");
}
__device__ __forceinline__ void wait_flag_kv(const unsigned* p) {
    unsigned spins = 0;
    while (((unsigned)__builtin_amdgcn_readfirstlane(ld_flag(p)) & 3u) != 2u) { dly(2); if (++spins > SPIN_CAP) break; }
    asm volatile("" ::: "memory");
}
__device__ __forceinline__ void chain_body(unsigned char* smem, const u16* __restrict__ Wq, const u16* __restrict__ Wk, const u16* __restrict__ Wv,
                                           const u16* __restrict__ Wo, const u16* __restrict__ mem0,
                                           const u16* Kx, const u16* Vx, u16* kmem, u16* vmem, unsigned char* ws_sync) {
    LAS unsigned char* L = (LAS unsigned char*)smem;
    unsigned* flags = (unsigned*)(ws_sync + WS_FLAGS);
    const auto rK = mk_rsrc(Kx, (unsigned)S_LEN * HD * 2);
    const auto rV = mk_rsrc(Vx, (unsigned)S_LEN * HD * 2);
    const auto rKM = mk_rsrc(kmem, (unsigned)NSEG * NH * MM * DH * 2);
    const auto rVM = mk_rsrc(vmem, (unsigned)NSEG * NH * MM * DH * 2);
    const int h = blockIdx.x, t = threadIdx.x, w = __builtin_amdgcn_readfirstlane(t >> 6), l = t & 63, g = l >> 4, c16 = l & 15;
    const auto rRS = mk_rsrc(ws_sync + WS_RS, 2u * 32 * 32 * 512);
    const auto rAG = mk_rsrc(ws_sync + WS_AG, 2u * 8192);
    const int sidx = (c16 < 8) ? 0x4444 : 0xEEEE;

    bf16x16 Bq[2][8], Bk[2][8], Bo[8][2];
    {
        constexpr int ST = C_MEM;
        v4u P[4][4];
        auto gl = [&](int r, v4u (&d)[4]) {
#pragma unroll
            for (int i = 0; i < 4; ++i) {
                const int u = i * 256 + t, k = u >> 4, ch = u & 15;
                if (r < 24) {
                    const u16* W = r < 8 ? Wq : (r < 16 ? Wk : Wv);
                    d[i] = *(const v4u*)(W + (size_t)(64 * (r & 7) + k) * HD + h * DH + ch * 8);
                } else {
                    const int kk = (r - 24) >> 2, cb = (r - 24) & 3;
                    const int gg = k >> 4, wi = k & 15, ii = 2 * (wi >> 2) + (wi & 1), cc = (wi >> 1) & 1;
                    const int e = 64 * kk + 32 * cc + 8 * gg + ii;
                    d[i] = *(const v4u*)(Wo + ((size_t)h * DH + e) * DM + cb * 128 + ch * 8);
                }
            }
        };
#pragma unroll
        for (int r = 0; r < 4; ++r) gl(r, P[r]);
#pragma unroll
        for (int r = 0; r < 32; ++r) {
            __syncthreads();
#pragma unroll
            for (int i = 0; i < 4; ++i) { const int u = i * 256 + t; *(LAS v4u*)(L + ST + (u >> 4) * 288 + (u & 15) * 16) = P[r & 3][i]; }
            if (r + 4 < 32) gl(r + 4, P[r & 3]);
            __syncthreads();
            if (r < 24) {
                const int mat = r >> 3, ks = r & 7;
#pragma unroll
                for (int tt = 0; tt < 2; ++tt) {
                    const LAS unsigned char* p = L + ST + (8 * g + (c16 >> 2)) * 288 + (32 * w + 16 * tt + (c16 & 3) * 4) * 2;
                    const bf16x8 lo = cat8(tr_read(p), tr_read(p + 4 * 288));
                    const bf16x8 hi = cat8(tr_read(p + 32 * 288), tr_read(p + 36 * 288));
                    if (mat == 0) Bq[tt][ks] = cat16(lo, hi);
                    else if (mat == 1) Bk[tt][ks] = cat16(lo, hi);
                    else {
                        *(LAS bf16x8*)(L + C_WV + (((2 * w + tt) * 8 + ks) * 2 + 0) * 1024 + l * 16) = lo;
                        *(LAS bf16x8*)(L + C_WV + (((2 * w + tt) * 8 + ks) * 2 + 1) * 1024 + l * 16) = hi;
                    }
                }
            } else {
                const int kk = (r - 24) >> 2, cb = (r - 24) & 3;
                if (w == cb) {
#pragma unroll
                    for (int tt = 0; tt < 8; ++tt) {
                        const LAS unsigned char* p = L + ST + (8 * g + (c16 >> 2)) * 288 + (16 * tt + (c16 & 3) * 4) * 2;
                        const bf16x8 lo = cat8(tr_read(p), tr_read(p + 4 * 288));
                        const bf16x8 hi = cat8(tr_read(p + 32 * 288), tr_read(p + 36 * 288));
                        Bo[tt][kk] = cat16(lo, hi);
                    }
                }
            }
        }
        __syncthreads();
    }
    {
#pragma unroll
        for (int i = 0; i < 2; ++i) {
            const int u = i * 256 + t, ks = u >> 6, ll = u & 63, gg = ll >> 4, cc = (ll >> 3) & 1, m = ll & 7;
            const u16* src = mem0 + m * DM + 64 * ks + 16 * gg + 2 * cc;
            v4u v;
#pragma unroll
            for (int q = 0; q < 4; ++q) v[q] = *(const unsigned*)(src + 4 * q);
            *(LAS v4u*)(L + C_MEM + u * 16) = v;
        }
        wait_flag_kv(flags + FL_QKV + h * 16);
#pragma unroll
        for (int i = 0; i < 2; ++i) {
            const int u = i * 256 + t, r = u >> 4, ch = u & 15;
            *(LAS v4u*)(L + C_KX + r * RST + ch * 16) = ld16_sc1(rK, (unsigned)(r * HD + h * DH + ch * 8) * 2u);
            *(LAS v4u*)(L + C_VX + r * RST + ch * 16) = ld16_sc1(rV, (unsigned)(r * HD + h * DH + ch * 8) * 2u);
        }
    }
    __syncthreads();

#define PIN(x) asm volatile("" : "+v"(x))
    for (int s = 0; s < NSEG; ++s) {
        int t = threadIdx.x; PIN(t);
        const int l = t & 63, g = l >> 4, c16 = l & 15;
        unsigned oWV = (unsigned)(size_t)(L + C_WV + (2 * w * 16) * 1024 + l * 16);
        unsigned oMEM = (unsigned)(size_t)(L + C_MEM + l * 16);
        unsigned oKX = (unsigned)(size_t)(L + C_KX + c16 * RST + 16 * g);
        unsigned oQK = (unsigned)(size_t)(L + C_QS + (c16 & 7) * RST + 16 * g);
        unsigned oQW = (unsigned)(size_t)(L + C_QS + 4 * (g & 1) * RST + (32 * w + 16 * (l >> 5) + c16) * 2);
        unsigned oVW = (unsigned)(size_t)(L + C_VS + (32 * w + 16 * (l >> 5) + c16) * 16 + (g & 1) * 8);
        PIN(oWV); PIN(oMEM); PIN(oKX); PIN(oQK); PIN(oQW); PIN(oVW);
#define LP(o) ((LAS unsigned char*)(size_t)(o))
        f32x4 aq[2], ak[2], av[2];
#pragma unroll
        for (int i = 0; i < 2; ++i) { aq[i] = (f32x4){0.f, 0.f, 0.f, 0.f}; ak[i] = aq[i]; av[i] = aq[i]; PIN(aq[i]); PIN(ak[i]); PIN(av[i]); }
        bf16x8 A[8], Vb[2][4];
#pragma unroll
        for (int i = 0; i < 4; ++i) Vb[0][i] = *(const LAS bf16x8*)(LP(oWV) + ((i >> 1) * 16 + (i & 1)) * 1024);
        if (s > 0) {
            const unsigned slot = (s - 1) & 1;
            dly(DLY2);
            v4u x[2];
            unsigned spins = 0;
            for (;;) {
                asm volatile("" ::: "memory");
                bool ok = true;
#pragma unroll
                for (int i = 0; i < 2; ++i) x[i] = ld16_sc1(rAG, slot * 8192u + (i * 256 + t) * 16u);
#pragma unroll
                for (int i = 0; i < 2; ++i) ok &= (x[i][0] != SENT) & (x[i][1] != SENT) & (x[i][2] != SENT) & (x[i][3] != SENT);
                if (__all(ok)) break;
                if (++spins > SPIN_CAP) break;
            }
#pragma unroll
            for (int i = 0; i < 2; ++i) *(LAS v4u*)(LP(oMEM) + (i * 256 + w * 64) * 16) = x[i];
            __syncthreads();
            if (t == 0) __hip_atomic_fetch_add(flags + FL_PROG, 1u, __ATOMIC_RELAXED, __HIP_MEMORY_SCOPE_AGENT);
        }
#pragma unroll
        for (int ks = 0; ks < 2; ++ks) A[ks] = *(const LAS bf16x8*)(LP(oMEM) + ks * 1024);
        SBAR();
#pragma unroll
        for (int ks = 0; ks < 8; ++ks) {
            if (ks < 6) A[ks + 2] = *(const LAS bf16x8*)(LP(oMEM) + (ks + 2) * 1024);
            if (ks + 1 < 8) {
#pragma unroll
                for (int i = 0; i < 4; ++i) Vb[(ks + 1) & 1][i] = *(const LAS bf16x8*)(LP(oWV) + ((i >> 1) * 16 + (ks + 1) * 2 + (i & 1)) * 1024);
            }
            SBAR();
            aq[0] = SMFMAC(A[ks], Bq[0][ks], aq[0], sidx);
            aq[1] = SMFMAC(A[ks], Bq[1][ks], aq[1], sidx);
            ak[0] = SMFMAC(A[ks], Bk[0][ks], ak[0], sidx);
            ak[1] = SMFMAC(A[ks], Bk[1][ks], ak[1], sidx);
            av[0] = SMFMAC(A[ks], cat16(Vb[ks & 1][0], Vb[ks & 1][1]), av[0], sidx);
            av[1] = SMFMAC(A[ks], cat16(Vb[ks & 1][2], Vb[ks & 1][3]), av[1], sidx);
            SBAR();
        }
        bf16x8 kx[2][4];
#pragma unroll
        for (int kq = 0; kq < 4; ++kq) {
            kx[0][kq] = *(const LAS bf16x8*)(LP(oKX) + 64 * kq);
            kx[1][kq] = *(const LAS bf16x8*)(LP(oKX) + 16 * RST + 64 * kq);
        }
        {
            const f32x4 q = comb2(aq[0], aq[1]), k = comb2(ak[0], ak[1]), v = comb2(av[0], av[1]);
            const unsigned q01 = pk2(q[0], q[1]), q23 = pk2(q[2], q[3]), k01 = pk2(k[0], k[1]), k23 = pk2(k[2], k[3]);
            LAS unsigned char* pq = LP(oQW);
            LAS unsigned char* pk = LP(oQW) + (C_KS - C_QS);
            *(LAS u16*)(pq) = (u16)q01; *(LAS u16*)(pq + RST) = (u16)(q01 >> 16); *(LAS u16*)(pq + 2 * RST) = (u16)q23; *(LAS u16*)(pq + 3 * RST) = (u16)(q23 >> 16);
            *(LAS u16*)(pk) = (u16)k01; *(LAS u16*)(pk + RST) = (u16)(k01 >> 16); *(LAS u16*)(pk + 2 * RST) = (u16)k23; *(LAS u16*)(pk + 3 * RST) = (u16)(k23 >> 16);
            v2u vv = {pk2(v[0], v[1]), pk2(v[2], v[3])};
            *(LAS v2u*)(LP(oVW)) = vv;
        }
        unsigned oVX = (unsigned)(size_t)(L + C_VX + (4 * g + (c16 >> 2)) * RST + (c16 & 3) * 8 + 64 * w);
        unsigned oVM = (unsigned)(size_t)(L + C_VS + c16 * 16 + (g & 1) * 8 + 512 * w);
        unsigned oOW = (unsigned)(size_t)(L + C_MEM + (c16 & 7) * RST + (32 * w + 4 * g) * 2);
        unsigned oOR = (unsigned)(size_t)(L + C_MEM + (c16 & 7) * RST + (32 * (c16 >> 3) + 8 * g) * 2);
        PIN(oVX); PIN(oVM); PIN(oOW); PIN(oOR);
        f32x4 oo[8];
        __syncthreads();
        if (s == NSEG - 1) break;
        bf16x8 aO[2];
        {
            bf16x8 qb[4], km_[4];
#pragma unroll
            for (int kq = 0; kq < 4; ++kq) {
                qb[kq] = *(const LAS bf16x8*)(LP(oQK) + 64 * kq);
                km_[kq] = *(const LAS bf16x8*)(LP(oQK) + (C_KS - C_QS) + 64 * kq);
            }
            bf16x8 va[2]; v2u vm[2];
#pragma unroll
            for (int T = 0; T < 2; ++T) {
                va[T] = cat8(tr_read(LP(oVX) + 32 * T), tr_read(LP(oVX) + 32 * T + 16 * RST));
                vm[T] = *(const LAS v2u*)(LP(oVM) + 256 * T);
            }
            SBAR();
            f32x4 sc[3];
#pragma unroll
            for (int T = 0; T < 3; ++T) sc[T] = (f32x4){0.f, 0.f, 0.f, 0.f};
#pragma unroll
            for (int kq = 0; kq < 4; ++kq) {
                sc[0] = MFMA32(kx[0][kq], qb[kq], sc[0]); sc[1] = MFMA32(kx[1][kq], qb[kq], sc[1]); sc[2] = MFMA32(km_[kq], qb[kq], sc[2]);
            }
            SBAR();
            const float cs = 0.08838834764831845f * 1.4426950408889634f;
            if (g >= 2) sc[2] = (f32x4){-3.0e38f, -3.0e38f, -3.0e38f, -3.0e38f};
            float mx = fmaxf(fmaxf(fmaxf(sc[0][0], sc[0][1]), fmaxf(sc[0][2], sc[0][3])), fmaxf(fmaxf(sc[1][0], sc[1][1]), fmaxf(sc[1][2], sc[1][3])));
            mx = fmaxf(mx, fmaxf(fmaxf(sc[2][0], sc[2][1]), fmaxf(sc[2][2], sc[2][3])));
            mx = xl32_max(xl16_max(mx));
            const float nm = -mx * cs;
            float p[12];
#pragma unroll
            for (int T = 0; T < 3; ++T)
#pragma unroll
                for (int r = 0; r < 4; ++r) p[T * 4 + r] = __builtin_amdgcn_exp2f(__builtin_fmaf(sc[T][r], cs, nm));
            float sum = ((p[0] + p[1]) + (p[2] + p[3])) + ((p[4] + p[5]) + (p[6] + p[7])) + ((p[8] + p[9]) + (p[10] + p[11]));
            sum = xl32_add(xl16_add(sum));
            const float inv = __builtin_amdgcn_rcpf(sum);
            v4u px = {pk2(p[0] * inv, p[1] * inv), pk2(p[2] * inv, p[3] * inv), pk2(p[4] * inv, p[5] * inv), pk2(p[6] * inv, p[7] * inv)};
            const bf16x8 Px = __builtin_bit_cast(bf16x8, px);
            const bf16x8 Pm = __builtin_bit_cast(bf16x8, (v4u){pk2(p[8] * inv, p[9] * inv), pk2(p[10] * inv, p[11] * inv), 0u, 0u});
            f32x4 o[2];
#pragma unroll
            for (int T = 0; T < 2; ++T) o[T] = MFMA32(va[T], Px, ((f32x4){0.f, 0.f, 0.f, 0.f}));
#pragma unroll
            for (int T = 0; T < 2; ++T) o[T] = MFMA32(__builtin_bit_cast(bf16x8, (v4u){vm[T][0], vm[T][1], 0u, 0u}), Pm, o[T]);
            if (c16 < 8) {
#pragma unroll
                for (int T = 0; T < 2; ++T) *(LAS v2u*)(LP(oOW) + 32 * T) = (v2u){pk2(o[T][0], o[T][1]), pk2(o[T][2], o[T][3])};
            }
#pragma unroll
            for (int tt = 0; tt < 8; ++tt) { oo[tt] = (f32x4){0.f, 0.f, 0.f, 0.f}; PIN(oo[tt]); }
            __syncthreads();
            aO[0] = *(const LAS bf16x8*)(LP(oOR));
            aO[1] = *(const LAS bf16x8*)(LP(oOR) + 128);
        }
        const unsigned slot = s & 1, par = (s >> 1) & 1;
        {
#pragma unroll
            for (int tt = 0; tt < 8; ++tt) oo[tt] = SMFMAC(aO[0], Bo[tt][0], oo[tt], sidx);
#pragma unroll
            for (int tt = 0; tt < 8; ++tt) oo[tt] = SMFMAC(aO[1], Bo[tt][1], oo[tt], sidx);
#pragma unroll
            for (int tp = 0; tp < 4; ++tp) {
                const f32x4 o = comb2(oo[2 * tp], oo[2 * tp + 1]);
                v4u st;
#pragma unroll
                for (int r = 0; r < 4; ++r) st[r] = (__float_as_uint(o[r]) & ~1u) | par;
                const unsigned cons = 8 * w + 2 * tp + (l >> 5);
                st16_sc1(st, rRS, slot * 524288u + cons * 16384u + h * 512u + (l & 31) * 16u);
            }
        }
        {
            const int u = t & 127;
            const v4u pub = *(const LAS v4u*)(L + (t < 128 ? C_KS + (u >> 4) * RST + (u & 15) * 16 : C_VS + u * 16));
            const unsigned off = (unsigned)((s * NH + h) * (MM * DH) + u * 8) * 2u;
            if (t < 128) st16_sc1(pub, rKM, off); else st16_sc1(pub, rVM, off);
        }
        if (((s + 1) & 3) == 0) wait_flag_kv(flags + FL_QKV + (((s + 1) >> 2) * 32 + h) * 16);
        v4u pk_[2], pv_[2];
#pragma unroll
        for (int i = 0; i < 2; ++i) {
            const int u = i * 256 + t, r = u >> 4, ch = u & 15;
            pk_[i] = ld16_sc1(rK, (unsigned)(((s + 1) * SEG + r) * HD + h * DH + ch * 8) * 2u);
            pv_[i] = ld16_sc1(rV, (unsigned)(((s + 1) * SEG + r) * HD + h * DH + ch * 8) * 2u);
        }
        const int lr = (l >> 3) & 3;
        unsigned offAG;
        {
            const int m = 4 * (w >> 1) + lr, cp = 4 * (w & 1) + ((l & 7) >> 1);
            offAG = slot * 8192u + (h >> 2) * 1024u + ((h & 3) * 16 + (cp & 1) * 8 + m) * 16u + (cp >> 1) * 4u;
            PIN(offAG);
        }
        dly(DLY1);
        {
            v4u x[4];
            const unsigned base = slot * 524288u + h * 16384u + (l >> 3) * 512u + (8 * w + (l & 7)) * 16u;
            unsigned spins = 0;
            for (;;) {
                asm volatile("" ::: "memory");
                unsigned bad = 0;
#pragma unroll
                for (int i = 0; i < 4; ++i) x[i] = ld16_sc1(rRS, base + i * 4096u);
#pragma unroll
                for (int i = 0; i < 4; ++i) bad |= (x[i][0] ^ par) | (x[i][1] ^ par) | (x[i][2] ^ par) | (x[i][3] ^ par);
                if (__all((bad & 1u) == 0)) break;
                if (++spins > SPIN_CAP) break;
            }
            float sm[4];
#pragma unroll
            for (int r = 0; r < 4; ++r) {
                float v = (__uint_as_float(x[0][r]) + __uint_as_float(x[1][r])) + (__uint_as_float(x[2][r]) + __uint_as_float(x[3][r]));
                sm[r] = xl32_add(xl16_add(xl8_add(v)));
            }
            const float own = lr == 0 ? sm[0] : (lr == 1 ? sm[1] : (lr == 2 ? sm[2] : sm[3]));
            const float nb = xl1_get(own);
            unsigned pkd = pk2(own, nb);
            if (pkd == SENT) pkd = 0x7FC07FC0u;
            if (l < 32 && !(l & 1)) {
                st4_sc1(pkd, rAG, offAG);
                st4_sc1(SENT, rAG, offAG ^ 8192u);
            }
        }
#pragma unroll
        for (int i = 0; i < 2; ++i) {
            const int u = i * 256 + t, r = u >> 4, ch = u & 15;
            *(LAS v4u*)(L + C_KX + r * RST + ch * 16) = pk_[i];
            *(LAS v4u*)(L + C_VX + r * RST + ch * 16) = pv_[i];
        }
    }
    {
        const int u = t & 127;
        const v4u pub = *(const LAS v4u*)(L + (t < 128 ? C_KS + (u >> 4) * RST + (u & 15) * 16 : C_VS + u * 16));
        const unsigned off = (unsigned)(((NSEG - 1) * NH + h) * (MM * DH) + u * 8) * 2u;
        if (t < 128) st16_sc1(pub, rKM, off); else st16_sc1(pub, rVM, off);
        asm volatile("s_waitcnt vmcnt(0)" ::: "memory");
        __syncthreads();
        if (t == 0) __hip_atomic_fetch_add(flags + FL_PROG, 1u, __ATOMIC_RELAXED, __HIP_MEMORY_SCOPE_AGENT);
    }
}

__device__ __forceinline__ void xattn_task(unsigned char* smem, const u16* Q, const u16* Kx, const u16* Vx,
                                           const u16* kmem, const u16* vmem, u16* __restrict__ O, int s, int h, const unsigned* prog) {
    const int t = threadIdx.x, w = t >> 6, l = t & 63, g = l >> 4, c16 = l & 15;
    LAS unsigned char* LV = (LAS unsigned char*)smem + w * 32 * 288;
    LAS unsigned char* LO = (LAS unsigned char*)smem + 4 * 32 * 288 + w * 32 * 272;
    const size_t rowb = (size_t)s * SEG;
#pragma unroll
    for (int i = 0; i < 8; ++i) {
        const int u = i * 64 + l, r = u >> 4, ch = u & 15;
        *(LAS v4u*)(LV + r * 288 + ch * 16) = *(const v4u*)(Vx + (rowb + r) * HD + h * DH + ch * 8);
    }
    f32x4 sc[2][3];
#pragma unroll
    for (int qt = 0; qt < 2; ++qt)
#pragma unroll
        for (int T = 0; T < 3; ++T) sc[qt][T] = (f32x4){0.f, 0.f, 0.f, 0.f};
    bf16x8 q0[4], q1[4];
#pragma unroll
    for (int kq = 0; kq < 4; ++kq) {
        const int eo = 32 * kq + 8 * g;
        q0[kq] = *(const bf16x8*)(Q + (rowb + c16) * HD + h * DH + eo);
        q1[kq] = *(const bf16x8*)(Q + (rowb + 16 + c16) * HD + h * DH + eo);
        const bf16x8 k0 = *(const bf16x8*)(Kx + (rowb + c16) * HD + h * DH + eo);
        const bf16x8 k1 = *(const bf16x8*)(Kx + (rowb + 16 + c16) * HD + h * DH + eo);
        sc[0][0] = MFMA32(k0, q0[kq], sc[0][0]); sc[0][1] = MFMA32(k1, q0[kq], sc[0][1]);
        sc[1][0] = MFMA32(k0, q1[kq], sc[1][0]); sc[1][1] = MFMA32(k1, q1[kq], sc[1][1]);
    }
    if (w == 0) {
        unsigned spins = 0;
        const int nap = s >= NSEG - 2 ? 4 : 40;
        while ((unsigned)__builtin_amdgcn_readfirstlane(ld_flag(prog)) < (unsigned)(s + 1) * NH) { dly(nap); if (++spins > SPIN_CAP) break; }
    }
    __syncthreads();
    const auto rKM = mk_rsrc(kmem, (unsigned)NSEG * NH * MM * DH * 2);
    const auto rVM = mk_rsrc(vmem, (unsigned)NSEG * NH * MM * DH * 2);
    v2u vmf[8];
#pragma unroll
    for (int T = 0; T < 8; ++T) vmf[T] = __builtin_amdgcn_raw_buffer_load_b64(rVM, (unsigned)((s * NH + h) * (MM * DH) + (16 * T + c16) * 8 + (g & 1) * 4) * 2u, 0, 16);
#pragma unroll
    for (int kq = 0; kq < 4; ++kq) {
        const bf16x8 k2 = __builtin_bit_cast(bf16x8, ld16_sc1(rKM, (unsigned)(((s * NH + h) * MM + (c16 & 7)) * DH + 32 * kq + 8 * g) * 2u));
        sc[0][2] = MFMA32(k2, q0[kq], sc[0][2]); sc[1][2] = MFMA32(k2, q1[kq], sc[1][2]);
    }
    bf16x8 Px[2], Pm[2];
    const float scale = 0.08838834764831845f;
#pragma unroll
    for (int qt = 0; qt < 2; ++qt) {
        const int qi = 16 * qt + c16;
        float p[12]; float mx = -3.0e38f;
#pragma unroll
        for (int T = 0; T < 3; ++T)
#pragma unroll
            for (int r = 0; r < 4; ++r) {
                float v = rbf(rbf(sc[qt][T][r]) * scale);
                const bool valid = (T == 2) ? (g < 2) : (16 * T + 4 * g + r <= qi);
                if (!valid) v = -3.0e38f;
                p[T * 4 + r] = v; mx = fmaxf(mx, v);
            }
        mx = fmaxf(mx, __shfl_xor(mx, 16)); mx = fmaxf(mx, __shfl_xor(mx, 32));
        float sum = 0.f;
#pragma unroll
        for (int i = 0; i < 12; ++i) { p[i] = (p[i] < -1.0e38f) ? 0.f : __expf(p[i] - mx); sum += p[i]; }
        sum += __shfl_xor(sum, 16); sum += __shfl_xor(sum, 32);
        const float inv = 1.0f / sum;
        v4u px = {pk2(p[0] * inv, p[1] * inv), pk2(p[2] * inv, p[3] * inv), pk2(p[4] * inv, p[5] * inv), pk2(p[6] * inv, p[7] * inv)};
        v2u pm = {pk2(p[8] * inv, p[9] * inv), pk2(p[10] * inv, p[11] * inv)};
        Px[qt] = __builtin_bit_cast(bf16x8, px); Pm[qt] = __builtin_bit_cast(bf16x8, (v4u){pm[0], pm[1], 0u, 0u});
    }
#pragma unroll
    for (int T = 0; T < 8; ++T) {
        const LAS unsigned char* pv = LV + (4 * g + (c16 >> 2)) * 288 + (16 * T + (c16 & 3) * 4) * 2;
        const bf16x8 va = cat8(tr_read(pv), tr_read(pv + 16 * 288));
        v2u vm = vmf[T];
        if (g >= 2) vm = (v2u){0u, 0u};
#pragma unroll
        for (int qt = 0; qt < 2; ++qt) {
            f32x4 o = (f32x4){0.f, 0.f, 0.f, 0.f};
            o = MFMA32(va, Px[qt], o);
            o = MFMA32(__builtin_bit_cast(bf16x8, (v4u){vm[0], vm[1], 0u, 0u}), Pm[qt], o);
            v2u st = {pk2(o[0], o[1]), pk2(o[2], o[3])};
            *(LAS v2u*)(LO + (16 * qt + c16) * 272 + (16 * T + 4 * g) * 2) = st;
        }
    }
    const auto rO = mk_rsrc(O, (unsigned)S_LEN * HD * 2);
#pragma unroll
    for (int i = 0; i < 8; ++i) {
        const int u = i * 64 + l, r = u >> 4, ch = u & 15;
        st16_sc1(*(const LAS v4u*)(LO + r * 272 + ch * 16), rO, (unsigned)((rowb + r) * HD + h * DH + ch * 8) * 2u);
    }
}

constexpr int NBULK = 224;
#define NRB_IN 12
struct Params {
    const u16 *x, *mem0, *Wq, *Wk, *Wv, *Wo, *wg, *wu, *wd;
    u16* out; unsigned char* ws;
};
__device__ __forceinline__ void bulk_wait(const unsigned* p, unsigned want) {
    if (threadIdx.x < 64) {
        unsigned spins = 0;
        while ((unsigned)__builtin_amdgcn_readfirstlane(ld_flag(p)) < want) { __builtin_amdgcn_s_sleep(48); if (++spins > SPIN_CAP) break; }
    }
    __syncthreads();
    __builtin_amdgcn_fence(__ATOMIC_ACQUIRE, "agent");
    asm volatile("s_waitcnt vmcnt(0)" ::: "memory");
}
__device__ __forceinline__ void bulk_arrive(unsigned* p) {
    asm volatile("s_waitcnt vmcnt(0)" ::: "memory");
    __syncthreads();
    if (threadIdx.x == 0) __hip_atomic_fetch_add(p, 1u, __ATOMIC_RELAXED, __HIP_MEMORY_SCOPE_AGENT);
}
__global__ void __launch_bounds__(256, 1) k_fused(Params P) {
    extern __shared__ __attribute__((aligned(16))) unsigned char smem[];
    unsigned char* ws = P.ws;
    u16* xn = (u16*)(ws + WS_XN); u16* Q = (u16*)(ws + WS_Q); u16* Kx = (u16*)(ws + WS_K); u16* Vx = (u16*)(ws + WS_V);
    u16* O = (u16*)(ws + WS_O); u16* kmem = (u16*)(ws + WS_KM); u16* vmem = (u16*)(ws + WS_VM);
    if (blockIdx.x < NH) {
        chain_body(smem, P.Wq, P.Wk, P.Wv, P.Wo, P.mem0, Kx, Vx, kmem, vmem, ws);
        return;
    }
    u16* H = (u16*)(ws + WS_H); u16* HN = (u16*)(ws + WS_HN); u16* ACT = (u16*)(ws + WS_ACT);
    unsigned* flags = (unsigned*)(ws + WS_FLAGS);
    const int b = blockIdx.x - NH, t = threadIdx.x, w = __builtin_amdgcn_readfirstlane(t >> 6), l = t & 63;
    for (int i = b; i < 16 * 96; i += NBULK) {
        int rb, hh, mat;
        if (i < 1024) { rb = i >> 6; hh = (i >> 1) & 31; mat = 1 + (i & 1); }
        else { const int j = i - 1024; rb = j >> 5; hh = j & 31; mat = 0; }
        const u16* W = mat == 0 ? P.Wq : (mat == 1 ? P.Wk : P.Wv);
        u16* C = mat == 0 ? Q : (mat == 1 ? Kx : Vx);
        gemm_tile<0, true, 128, 128>(smem, xn, DM, W, nullptr, HD, C, HD, nullptr, DM, rb * 128, hh * DH);
        asm volatile("s_waitcnt vmcnt(0)" ::: "memory");
        __syncthreads();
        if (t == 0) __hip_atomic_fetch_add(flags + FL_QKV + (rb * 32 + hh) * 16, mat == 0 ? 4u : 1u, __ATOMIC_RELAXED, __HIP_MEMORY_SCOPE_AGENT);
    }
    float* slab = (float*)(ws + WS_SLAB);
    const int role = b < 48 ? 0 : (b < 112 ? 1 : (b < 128 ? 2 : (b < 192 ? 3 : 4)));
    const int rb0 = role == 0 ? 0 : (role == 1 ? 48 : (role == 2 ? 112 : (role == 3 ? 128 : 192)));
    const int nrole = role == 0 ? 48 : (role == 1 ? 64 : (role == 2 ? 16 : (role == 3 ? 64 : 32)));
    const int ntask = role == 0 ? 32 : (role == 1 ? 16 : (role == 2 ? 16 : (role == 3 ? 28 : 8)));
    const int nrb = role == 0 ? 16 : NRB_IN;
    for (int idx = b - rb0; idx < nrb * ntask; idx += nrole) {
        const int rb = idx / ntask, r = idx % ntask, m0 = rb * 128;
        unsigned* gf = flags + FL_GRP + rb * 4 * 16;
        if (role == 0) {
            const int s = 4 * rb + (r >> 3), hh = (r & 7) * 4 + w;
            wait_flag_ge(flags + FL_QKV + (rb * 32 + hh) * 16, 6, 32);
            __builtin_amdgcn_fence(__ATOMIC_ACQUIRE, "agent");
            asm volatile("s_waitcnt vmcnt(0)" ::: "memory");
            __syncthreads();
            xattn_task(smem, Q, Kx, Vx, kmem, vmem, O, s, hh, flags + FL_PROG);
            bulk_arrive(gf + 0 * 16);
        } else if (role == 1) {
            const int ks = r >> 2, nt = r & 3;
            bulk_wait(gf + 0 * 16, 32);
            gemm_tile<4, true, 128, 128>(smem, O + ks * 1024, HD, P.Wo + (size_t)ks * 1024 * DM, nullptr, DM, (u16*)(slab + (size_t)ks * S_LEN * DM), DM, nullptr, 1024, m0, nt * 128);
            bulk_arrive(gf + 1 * 16);
        } else if (role == 2) {
            bulk_wait(gf + 1 * 16, 16);
#pragma unroll
            for (int rr = 0; rr < 2; ++rr) {
                const int row = m0 + r * 8 + w * 2 + rr;
                f32x4 a0 = (f32x4){0.f, 0.f, 0.f, 0.f}, a1 = a0;
#pragma unroll
                for (int ks = 0; ks < 4; ++ks) {
                    const float* src = slab + ((size_t)ks * S_LEN + row) * DM + l * 8;
                    a0 += *(const f32x4*)src; a1 += *(const f32x4*)(src + 4);
                }
                const v4u xr = *(const v4u*)(P.x + (size_t)row * DM + l * 8);
                float v[8];
#pragma unroll
                for (int i = 0; i < 4; ++i) {
                    const float lo = i < 2 ? a0[2 * i] : a1[2 * i - 4], hi = i < 2 ? a0[2 * i + 1] : a1[2 * i - 3];
                    v[2 * i] = rbf(__uint_as_float(xr[i] << 16) + rbf(lo)); v[2 * i + 1] = rbf(__uint_as_float(xr[i] & 0xffff0000u) + rbf(hi));
                }
                float sm = 0.f;
#pragma unroll
                for (int i = 0; i < 8; ++i) sm += v[i];
#pragma unroll
                for (int o = 32; o >= 1; o >>= 1) sm += __shfl_xor(sm, o);
                const float mean = sm * (1.0f / DM);
                float q = 0.f;
#pragma unroll
                for (int i = 0; i < 8; ++i) { const float d = v[i] - mean; q += d * d; }
#pragma unroll
                for (int o = 32; o >= 1; o >>= 1) q += __shfl_xor(q, o);
                const float mb = rbf(mean), den = rbf(sqrtf(rbf(rbf(q * (1.0f / DM)) + 1e-5f)));
                v2u h0 = {pk2(v[0], v[1]), pk2(v[2], v[3])}, h1 = {pk2(v[4], v[5]), pk2(v[6], v[7])};
                v2u n0 = {pk2(rbf(v[0] - mb) / den, rbf(v[1] - mb) / den), pk2(rbf(v[2] - mb) / den, rbf(v[3] - mb) / den)};
                v2u n1 = {pk2(rbf(v[4] - mb) / den, rbf(v[5] - mb) / den), pk2(rbf(v[6] - mb) / den, rbf(v[7] - mb) / den)};
                st8_dev(H + (size_t)row * DM + l * 8, h0); st8_dev(H + (size_t)row * DM + l * 8 + 4, h1);
                st8_dev(HN + (size_t)row * DM + l * 8, n0); st8_dev(HN + (size_t)row * DM + l * 8 + 4, n1);
            }
            bulk_arrive(gf + 2 * 16);
        } else if (role == 3) {
            bulk_wait(gf + 2 * 16, 16);
            gemm_tile<2, true, 128, 128>(smem, HN, DM, P.wg, P.wu, FF, ACT, FF, nullptr, DM, m0, r * 64);
            bulk_arrive(gf + 3 * 16);
        } else {
            bulk_wait(gf + 3 * 16, 28);
            gemm_tile<1, false, 128, 64>(smem, ACT, FF, P.wd, nullptr, DM, P.out, DM, H, FF, m0, r * 64);
        }
    }
}

extern "C" void kernel_launch(void* const* d_in, const int* in_sizes, int n_in, void* d_out, int out_size, void* d_ws, size_t ws_size,
                              hipStream_t stream) {
    const u16* x = (const u16*)d_in[0];
    const u16* mem0 = (const u16*)d_in[1];
    const u16* Wq = (const u16*)d_in[2];
    const u16* Wk = (const u16*)d_in[3];
    const u16* Wv = (const u16*)d_in[4];
    const u16* Wo = (const u16*)d_in[5];
    const u16* wg = (const u16*)d_in[8];
    const u16* wu = (const u16*)d_in[9];
    const u16* wd = (const u16*)d_in[10];
    u16* out = (u16*)d_out;
    unsigned char* ws = (unsigned char*)d_ws;
    u16* xn = (u16*)(ws + WS_XN); u16* Q = (u16*)(ws + WS_Q); u16* K = (u16*)(ws + WS_K); u16* V = (u16*)(ws + WS_V);
    u16* O = (u16*)(ws + WS_O); u16* km = (u16*)(ws + WS_KM); u16* vm = (u16*)(ws + WS_VM);
    u16* H = (u16*)(ws + WS_H); u16* HN = (u16*)(ws + WS_HN); u16* ACT = (u16*)(ws + WS_ACT);

    (void)hipFuncSetAttribute((const void*)k_fused, hipFuncAttributeMaxDynamicSharedMemorySize, C_LDS);
#define TBM 64
#define TBN 64
#define TBM1 64
    (void)hipFuncSetAttribute((const void*)k_gemm<2, TBM1, 128>, hipFuncAttributeMaxDynamicSharedMemorySize, 2 * g_stage(TBM1, 128));
    (void)hipFuncSetAttribute((const void*)k_gemm_splitk<TBM, TBN>, hipFuncAttributeMaxDynamicSharedMemorySize, 2 * g_stage(TBM, TBN));
    k_ln<true><<<S_LEN / 4, 256, 0, stream>>>(x, xn, ws);
    Params P{x, mem0, Wq, Wk, Wv, Wo, wg, wu, wd, out, ws};
    k_fused<<<NH + NBULK, 256, C_LDS, stream>>>(P);
    float* slab = (float*)(ws + WS_SLAB);
    constexpr int R0 = NRB_IN * 128, NR = S_LEN - R0;
    k_gemm_splitk<TBM, TBN><<<dim3(DM / TBN, NR / TBM, 8), 256, 2 * g_stage(TBM, TBN), stream>>>(O, HD, Wo, DM, slab, HD / 8, R0);
    k_reduce<8, true><<<NR / 4, 256, 0, stream>>>(slab, x, H, HN, R0);
    k_gemm<2, TBM1, 128><<<dim3(FF / 64, NR / TBM1), 256, 2 * g_stage(TBM1, 128), stream>>>(HN, DM, wg, wu, FF, ACT, FF, nullptr, DM, R0);
    k_gemm_splitk<TBM, TBN><<<dim3(DM / TBN, NR / TBM, 7), 256, 2 * g_stage(TBM, TBN), stream>>>(ACT, FF, wd, DM, slab, FF / 7, R0);
    k_reduce<7, false><<<NR / 4, 256, 0, stream>>>(slab, H, out, nullptr, R0);
}
```
